```python
import torch

def _softmax(x, axis=-1):
    return torch.softmax(x, dim=axis)
def _gelu(x):
    return torch.nn.functional.gelu(x, approximate="tanh")
def _silu(x):
    return torch.nn.functional.silu(x)
def _sigmoid(x):
    return torch.sigmoid(x)
def _layernorm(x, axis=-1, eps=1e-5):
    m = x.mean(dim=axis, keepdim=True)
    v = x.var(dim=axis, keepdim=True, unbiased=False)
    return (x - m) / torch.sqrt(v + eps)
def _scatter_add(tgt, idx, src, axis=0):
    return tgt.clone().index_add_(axis, idx, src)
def _segment_sum(src, seg, n):
    out = torch.zeros((n,) + src.shape[1:], dtype=src.dtype)
    return out.index_add_(0, seg, src)
def _pad(x, pw):
    flat = tuple(v for pair in reversed(pw) for v in pair)
    return torch.nn.functional.pad(x, flat)

kernel_name = 'interleave_layers__sliding_block_cached__mixer_channel_mlp__decode'

def setup_inputs():

    _w = (lambda a: (lambda t: t.to(torch.bfloat16) if t.is_floating_point() else t)(torch.as_tensor(a)))
    def _A_setup():
        B, Sq, D = 8, 1, 640
        Hq, Hkv, Sk, Dh = 32, 32, 4096, 128
        assert Hq % Hkv == 0, (Hq, Hkv)
        return {
            "x": _w((lambda *s: torch.randn(*s, dtype=torch.bfloat16) * 0.05)(B, Sq, D)),
            "k_cache": _w((lambda *s: torch.randn(*s, dtype=torch.bfloat16) * 3.2)(B, Hkv, Sk, Dh)),
            "v_cache": _w((lambda *s: torch.randn(*s, dtype=torch.bfloat16) * 0.4)(B, Hkv, Sk, Dh)),
            "wq": _w((lambda *s: torch.randn(*s, dtype=torch.bfloat16) * 3.2)(D, Hq * Dh) * (D ** -0.5)),
            "wo": _w((lambda *s: torch.randn(*s, dtype=torch.bfloat16) * 0.4)(Hq * Dh, D) * ((Hq * Dh) ** -0.5)),
        }
    _A_in = _A_setup()

    def _B_setup():
        B, S, D, F = 8, 1, 640, 1728
        return {
            "x": _w((lambda *s: torch.randn(*s, dtype=torch.bfloat16) * 0.05)(B, S, D)),
            "w1": _w((lambda *s: torch.randn(*s, dtype=torch.bfloat16) * 1.6)(D, F) * (D ** -0.5)),
            "b1": _w((lambda *s: torch.randn(*s, dtype=torch.bfloat16) * 1.6)(F)),
            "w2": _w((lambda *s: torch.randn(*s, dtype=torch.bfloat16) * 0.1)(F, D) * (F ** -0.5)),
            "b2": _w((lambda *s: torch.randn(*s, dtype=torch.bfloat16) * 0.1)(D)),
        }
    _B_in = _B_setup()

    return {
        "A_x": _A_in["x"],
        "A_k_cache": _A_in["k_cache"],
        "A_v_cache": _A_in["v_cache"],
        "A_wq": _A_in["wq"],
        "A_wo": _A_in["wo"],
        "B_x": _B_in["x"],
        "B_w1": _B_in["w1"],
        "B_b1": _B_in["b1"],
        "B_w2": _B_in["w2"],
        "B_b2": _B_in["b2"],
    }

def reference(A_x, A_k_cache, A_v_cache, A_wq, A_wo, B_x, B_w1, B_b1, B_w2, B_b2):
    def _A_ref(x, k_cache, v_cache, wq, wo):
        B, Sq, D = x.shape
        Hq, Dh = 32, 128
        Hkv, Sk = k_cache.shape[1], k_cache.shape[2]
        W = min(4096, max(256, Sk // 4))
        rep = Hq // Hkv
        q = (lambda x, *d: x.permute(*d))((x @ wq).reshape(B, Sq, Hq, Dh), 0, 2, 1, 3)
        k = (lambda x, n, axis: torch.repeat_interleave(x, n, dim=axis))(k_cache[:, :, Sk - W:, :], rep, axis=1)
        v = (lambda x, n, axis: torch.repeat_interleave(x, n, dim=axis))(v_cache[:, :, Sk - W:, :], rep, axis=1)
        scores = torch.einsum("bhqd,bhkd->bhqk", q, k) * (Dh ** -0.5)
        p = _softmax(scores, axis=-1)
        o = torch.einsum("bhqk,bhkd->bhqd", p, v)
        return (lambda x, *d: x.permute(*d))(o, 0, 2, 1, 3).reshape(B, Sq, Hq * Dh) @ wo

    def _B_ref(x, w1, b1, w2, b2):
        y = _layernorm(x, axis=-1)
        h = _gelu(torch.einsum("bsd,df->bsf", y, w1) + b1)
        return torch.einsum("bsf,fd->bsd", h, w2) + b2

    x = A_x
    x = x + _A_ref(_layernorm(x, axis=-1), A_k_cache, A_v_cache, A_wq, A_wo)
    x = x + _B_ref(_layernorm(x, axis=-1), B_w1, B_b1, B_w2, B_b2)
    x = x + _A_ref(_layernorm(x, axis=-1), A_k_cache, A_v_cache, A_wq, A_wo)
    x = x + _B_ref(_layernorm(x, axis=-1), B_w1, B_b1, B_w2, B_b2)
    return x
```

```cpp
#include <hip/hip_runtime.h>
#include <hip/hip_bf16.h>
#include <stdint.h>

typedef unsigned short u16;
__device__ __forceinline__ float bf2f(u16 v){ return __uint_as_float(((unsigned)v)<<16); }
__device__ __forceinline__ float rbf(float f){ return __bfloat162float(__float2bfloat16(f)); }

constexpr int B=8, D=640, H=32, SK=4096, DH=128, W=1024, F=1728;

__device__ float block_sum(float v, float* red){
  for(int o=32;o>0;o>>=1) v+=__shfl_xor(v,o);
  __syncthreads();
  if((threadIdx.x&63)==0) red[threadIdx.x>>6]=v;
  __syncthreads();
  float s=0; for(int i=0;i<(int)(blockDim.x>>6);i++) s+=red[i];
  return s;
}
__device__ float block_max(float v, float* red){
  for(int o=32;o>0;o>>=1) v=fmaxf(v,__shfl_xor(v,o));
  __syncthreads();
  if((threadIdx.x&63)==0) red[threadIdx.x>>6]=v;
  __syncthreads();
  float s=-INFINITY; for(int i=0;i<(int)(blockDim.x>>6);i++) s=fmaxf(s,red[i]);
  return s;
}
__device__ void ln_row(float* y, float* red){
  float s=0; for(int i=threadIdx.x;i<D;i+=blockDim.x) s+=y[i];
  float m=block_sum(s,red)/D;
  float v=0; for(int i=threadIdx.x;i<D;i+=blockDim.x){ float d=y[i]-m; v+=d*d; }
  v=block_sum(v,red)/D;
  float r=1.f/sqrtf(v+1e-5f);
  __syncthreads();
  for(int i=threadIdx.x;i<D;i+=blockDim.x) y[i]=rbf((y[i]-m)*r);
  __syncthreads();
}
__global__ void k_init(const u16* x, float* xf){ int i=blockIdx.x*256+threadIdx.x; if(i<B*D) xf[i]=bf2f(x[i]); }
__global__ void __launch_bounds__(256) k_attn(const float* xf, const u16* wq, const u16* kc, const u16* vc, float* o){
  __shared__ float y[D]; __shared__ float red[8]; __shared__ float q[DH]; __shared__ float p[W]; __shared__ float part[256];
  int b=blockIdx.x/H, h=blockIdx.x%H, t=threadIdx.x;
  for(int i=t;i<D;i+=256) y[i]=xf[b*D+i];
  __syncthreads();
  ln_row(y,red);
  { int d=t&127, half=t>>7; float a=0; for(int k=half*320;k<half*320+320;k++) a+=y[k]*bf2f(wq[(size_t)k*4096+h*128+d]); part[t]=a; }
  __syncthreads();
  if(t<128) q[t]=rbf(part[t]+part[t+128]);
  __syncthreads();
  const u16* K=kc+((size_t)(b*H+h)*SK+(SK-W))*DH;
  const u16* V=vc+((size_t)(b*H+h)*SK+(SK-W))*DH;
  float mx=-INFINITY;
  for(int j=t;j<W;j+=256){ float a=0; const uint4* kr=(const uint4*)(K+(size_t)j*DH);
    _Pragma("unroll 2") for(int c=0;c<16;c++){ uint4 u=kr[c]; unsigned w[4]={u.x,u.y,u.z,u.w};
      for(int e=0;e<4;e++){ a+=q[c*8+2*e]*__uint_as_float(w[e]<<16)+q[c*8+2*e+1]*__uint_as_float(w[e]&0xffff0000u);} }
    a*=0.08838834764831845f; p[j]=a; mx=fmaxf(mx,a);}
  mx=block_max(mx,red);
  float s=0; for(int j=t;j<W;j+=256){ float e=__expf(p[j]-mx); p[j]=e; s+=e; }
  s=block_sum(s,red);
  __syncthreads();
  { int d=t&127, half=t>>7; float a=0; for(int j=half*512;j<half*512+512;j++) a+=p[j]*bf2f(V[(size_t)j*DH+d]); part[t]=a; }
  __syncthreads();
  if(t<128) o[b*4096+h*128+t]=rbf((part[t]+part[t+128])/s);
}
__global__ void k_wo(const float* o, const u16* wo, float* xf){
  __shared__ float part[256];
  int b=blockIdx.x, c=blockIdx.y*64+(threadIdx.x&63), kq=threadIdx.x>>6;
  float a=0; for(int k=kq*1024;k<kq*1024+1024;k++) a+=o[b*4096+k]*bf2f(wo[(size_t)k*D+c]);
  part[threadIdx.x]=a; __syncthreads();
  if(threadIdx.x<64){ float s=part[threadIdx.x]+part[threadIdx.x+64]+part[threadIdx.x+128]+part[threadIdx.x+192]; xf[b*D+c]=rbf(xf[b*D+c]+rbf(s)); }
}
__global__ void k_mlp1(const float* xf, const u16* w1, const u16* b1, float* hh){
  __shared__ float y[D]; __shared__ float red[8]; __shared__ float part[256];
  int b=blockIdx.x, c=blockIdx.y*64+(threadIdx.x&63), kq=threadIdx.x>>6;
  for(int i=threadIdx.x;i<D;i+=256) y[i]=xf[b*D+i];
  __syncthreads();
  ln_row(y,red); ln_row(y,red);
  float a=0; for(int k=kq*160;k<kq*160+160;k++) a+=y[k]*bf2f(w1[(size_t)k*F+c]);
  part[threadIdx.x]=a; __syncthreads();
  if(threadIdx.x<64){ float s=part[threadIdx.x]+part[threadIdx.x+64]+part[threadIdx.x+128]+part[threadIdx.x+192];
    s=rbf(rbf(s)+bf2f(b1[c])); float g=0.5f*s*(1.f+tanhf(0.7978845608028654f*(s+0.044715f*s*s*s))); hh[b*F+c]=rbf(g); }
}
__global__ void k_mlp2(const float* hh, const u16* w2, const u16* b2, float* xf, __hip_bfloat16* out){
  __shared__ float part[256];
  int b=blockIdx.x, c=blockIdx.y*64+(threadIdx.x&63), kq=threadIdx.x>>6;
  float a=0; for(int k=kq*432;k<kq*432+432;k++) a+=hh[b*F+k]*bf2f(w2[(size_t)k*D+c]);
  part[threadIdx.x]=a; __syncthreads();
  if(threadIdx.x<64){ float s=part[threadIdx.x]+part[threadIdx.x+64]+part[threadIdx.x+128]+part[threadIdx.x+192];
    float r=rbf(xf[b*D+c]+rbf(rbf(s)+bf2f(b2[c]))); xf[b*D+c]=r; out[b*D+c]=__float2bfloat16(r); }
}
extern "C" void kernel_launch(void* const* d_in, const int* in_sizes, int n_in, void* d_out, int out_size, void* d_ws, size_t ws_size, hipStream_t stream){
  const u16* x=(const u16*)d_in[0]; const u16* kc=(const u16*)d_in[1]; const u16* vc=(const u16*)d_in[2];
  const u16* wq=(const u16*)d_in[3]; const u16* wo=(const u16*)d_in[4];
  const u16* w1=(const u16*)d_in[6]; const u16* b1=(const u16*)d_in[7]; const u16* w2=(const u16*)d_in[8]; const u16* b2=(const u16*)d_in[9];
  float* xf=(float*)d_ws; float* o=xf+8192; float* hh=o+8*4096;
  k_init<<<20,256,0,stream>>>(x,xf);
  for(int l=0;l<2;l++){
    k_attn<<<256,256,0,stream>>>(xf,wq,kc,vc,o);
    k_wo<<<dim3(8,10),256,0,stream>>>(o,wo,xf);
    k_mlp1<<<dim3(8,27),256,0,stream>>>(xf,w1,b1,hh);
    k_mlp2<<<dim3(8,10),256,0,stream>>>(hh,w2,b2,xf,(__hip_bfloat16*)d_out);
  }
}
```

```cpp
#include <hip/hip_runtime.h>
#include <stdint.h>

typedef unsigned u32;
typedef unsigned long long u64;
typedef u32 v4u __attribute__((ext_vector_type(4)));
typedef u32 v2u __attribute__((ext_vector_type(2)));
typedef float f32x4 __attribute__((ext_vector_type(4)));
typedef float f32x2 __attribute__((ext_vector_type(2)));
typedef short v4s __attribute__((ext_vector_type(4)));
typedef __bf16 bf16x2 __attribute__((ext_vector_type(2)));
#define LAS __attribute__((address_space(3)))
#define GAS __attribute__((address_space(1)))
#define DI __device__ __forceinline__
#define GAP() __builtin_amdgcn_sched_barrier(0)

constexpr u32 WS_EPOCH = 0, WS_FLAGS = 0x400, WS_DUMMY = 0x800;
constexpr u32 WS_Q = 0x1000, Q_L = 131072;
constexpr u32 WS_O = WS_Q + 2 * Q_L, O_L = 65536;
constexpr u32 WS_XP = WS_O + 2 * O_L, XP_L = 20480;
constexpr u32 WS_H = WS_XP + 2 * XP_L, H_L = 27648;
constexpr u32 WS_X2 = WS_H + 2 * H_L;
constexpr u32 WS_STAMP = 1u << 20;
constexpr u32 LDS_WAVE = 36864, LDS_SC = 4 * LDS_WAVE, SC_ATT = LDS_SC, SC_ML = SC_ATT + 4096, SC_ROLE = SC_ML + 256,
              SC_STAT = SC_ROLE + 4096, SC_CS = SC_STAT + 1024, LDS_BYTES = 163840;
constexpr u32 SENT = 0xFFFFFFFFu, ONES = 0x3F803F80u;
constexpr int SPIN_CAP = 1 << 17;

DI u32 pk_bf16(float lo, float hi) {
  f32x2 v = {lo, hi};
  return __builtin_bit_cast(u32, __builtin_convertvector(v, bf16x2));
}
DI float bf_lo(u32 x) { return __builtin_bit_cast(float, x << 16); }
DI float bf_hi(u32 x) { return __builtin_bit_cast(float, x & 0xffff0000u); }
DI u32 umax4(v4u a) { return max(max(a[0], a[1]), max(a[2], a[3])); }
template <int CTRL> DI float dppf(float x) {
  return __builtin_bit_cast(float, __builtin_amdgcn_mov_dpp(__builtin_bit_cast(int, x), CTRL, 0xf, 0xf, true));
}
DI float wave_max(float x) {
  x = fmaxf(x, dppf<0xB1>(x)); x = fmaxf(x, dppf<0x4E>(x)); x = fmaxf(x, dppf<0x124>(x)); x = fmaxf(x, dppf<0x128>(x));
  auto s = __builtin_amdgcn_permlane16_swap(__float_as_uint(x), __float_as_uint(x), false, false);
  x = fmaxf(__uint_as_float(s[0]), __uint_as_float(s[1]));
  auto t = __builtin_amdgcn_permlane32_swap(__float_as_uint(x), __float_as_uint(x), false, false);
  return fmaxf(__uint_as_float(t[0]), __uint_as_float(t[1]));
}
DI float wave_sum(float x) {
  x += dppf<0xB1>(x); x += dppf<0x4E>(x); x += dppf<0x124>(x); x += dppf<0x128>(x);
  auto s = __builtin_amdgcn_permlane16_swap(__float_as_uint(x), __float_as_uint(x), false, false);
  x = __uint_as_float(s[0]) + __uint_as_float(s[1]);
  auto t = __builtin_amdgcn_permlane32_swap(__float_as_uint(x), __float_as_uint(x), false, false);
  return __uint_as_float(t[0]) + __uint_as_float(t[1]);
}
DI void lds_barrier() { asm volatile("s_waitcnt lgkmcnt(0)\n\ts_barrier" ::: "memory"); }
DI void st_sc1(char* base, u32 off, u32 v) { asm volatile("global_store_dword %0, %1, %2 sc1" ::"v"(off), "v"(v), "s"(base) : "memory"); }
DI void st2_sc1(char* base, u32 off, v2u v) { asm volatile("global_store_dwordx2 %0, %1, %2 sc1" ::"v"(off), "v"(v), "s"(base) : "memory"); }
template <typename T> DI T lds_ld(u32 a) { return *(const LAS T*)(size_t)a; }
template <typename T> DI void lds_st(u32 a, T v) { *(LAS T*)(size_t)a = v; }
DI v4u tr_frag(u32 a, u32 second) {
  const v4s lo = __builtin_amdgcn_ds_read_tr16_b64_v4i16((LAS v4s*)(size_t)a);
  const v4s hi = __builtin_amdgcn_ds_read_tr16_b64_v4i16((LAS v4s*)(size_t)(a + second));
  const v2u l = __builtin_bit_cast(v2u, lo), h = __builtin_bit_cast(v2u, hi);
  return v4u{l[0], l[1], h[0], h[1]};
}
DI int lane_id() { int l; asm volatile("v_mbcnt_lo_u32_b32 %0, -1, 0\n\tv_mbcnt_hi_u32_b32 %0, -1, %0" : "=v"(l)); return l; }
#define LANES() const int lane = lane_id(), g = lane >> 4, n = lane & 15, eh = lane >> 5, eb = (lane >> 2) & 7, ec = lane & 3, ecol = 8 * eh + 2 * ec; (void)g; (void)n; (void)eb; (void)ecol
#define OPAQUE(p) asm volatile("" : "+s"(p))
#define MF "v_mfma_f32_16x16x32_bf16 "
#define VMWAIT(n) asm volatile("s_waitcnt vmcnt(%0)" ::"i"(n) : "memory")

#define LD4(C, A, i0, vo, base)                                                                                     \
  asm volatile("global_load_dwordx4 %0, %4, %5 nt\n\tglobal_load_dwordx4 %1, %4, %5 offset:1024 nt\n\t"               \
               "global_load_dwordx4 %2, %4, %5 offset:2048 nt\n\tglobal_load_dwordx4 %3, %4, %5 offset:3072 nt"       \
               : C(A[i0]), C(A[i0 + 1]), C(A[i0 + 2]), C(A[i0 + 3])                                                   \
               : "v"(vo), "s"(base)                                                                                   \
               : "memory")
#define DMA4(vo, base, m0v)                                                                                          \
  asm volatile("s_mov_b32 m0, %2\n\ts_nop 0\n\tglobal_load_lds_dwordx4 %0, %1 nt\n\t"                                  \
               "global_load_lds_dwordx4 %0, %1 offset:1024 nt\n\tglobal_load_lds_dwordx4 %0, %1 offset:2048 nt\n\t"    \
               "global_load_lds_dwordx4 %0, %1 offset:3072 nt" ::"v"(vo), "s"(base), "s"(m0v)                          \
               : "memory")

#define STAMP(i) do { } while (0)

struct St {
  v4u K[64], V[28];
  const char* vb;
  u32 voff, vrun, lbase, skip;
};
template <int J> DI void issue_v(St& s) {
  if constexpr (J < 9)
    asm volatile("s_cmp_lg_u32 %3, 0\n\ts_cbranch_scc1 .Lv%=\n\ts_add_u32 m0, %2, %4\n\ts_nop 0\n\tglobal_load_lds_dwordx4 %1, %0 nt\n\t"
                 "global_load_lds_dwordx4 %1, %0 offset:1024 nt\n\tglobal_load_lds_dwordx4 %1, %0 offset:2048 nt\n\t"
                 "global_load_lds_dwordx4 %1, %0 offset:3072 nt\n\tv_add_u32 %1, 0x4000, %1\n.Lv%=:"
                 : "+s"(s.vb), "+v"(s.vrun) : "s"(s.lbase), "s"(s.skip), "i"(J * 4096) : "memory", "scc");
  else
    asm volatile("s_cmp_lg_u32 %6, 0\n\ts_cbranch_scc1 .Lv%=\n\tglobal_load_dwordx4 %0, %5, %4 nt\n\tglobal_load_dwordx4 %1, %5, %4 offset:1024 nt\n\t"
                 "global_load_dwordx4 %2, %5, %4 offset:2048 nt\n\tglobal_load_dwordx4 %3, %5, %4 offset:3072 nt\n\tv_add_u32 %5, 0x4000, %5\n.Lv%=:"
                 : "+v"(s.V[(J - 9) * 4]), "+v"(s.V[(J - 9) * 4 + 1]), "+v"(s.V[(J - 9) * 4 + 2]), "+v"(s.V[(J - 9) * 4 + 3]), "+s"(s.vb), "+v"(s.vrun)
                 : "s"(s.skip) : "memory", "scc");
}
DI void mf_v(f32x4& d, const v4u& a, const v4u& b) { asm volatile(MF "%0, %1, %2, %0" : "+v"(d) : "v"(a), "v"(b)); }

#define ROT0 "quad_perm:[0,1,2,3]"
#define ROT1 "row_ror:4"
#define ROT2 "row_ror:8"
#define ROT3 "row_ror:12"
#define FOLD(ra, rb, da, db, rot, bank) "v_add_f32_dpp " ra ", " da ", " ra " " rot " row_mask:0xf bank_mask:" bank "\n\tv_add_f32_dpp " rb ", " db ", " rb " " rot " row_mask:0xf bank_mask:" bank "\n\t"
#define QKP(r0, r1, r2, r3, bank)                                                                                                     \
  asm volatile(MF "%0, %4, %12, 0\n\t" MF "%1, %5, %12, 0\n\t" MF "%0, %6, %13, %0\n\t" MF "%1, %7, %13, %1\n\t" MF "%0, %8, %14, %0\n\t" \
               FOLD("%2", "%3", "%16", "%20", r0, bank) MF "%1, %9, %14, %1\n\t" FOLD("%2", "%3", "%17", "%21", r1, bank)              \
               MF "%0, %10, %15, %0\n\t" FOLD("%2", "%3", "%18", "%22", r2, bank) MF "%1, %11, %15, %1\n\t" FOLD("%2", "%3", "%19", "%23", r3, bank) \
               : "=&v"(d[c][0]), "=&v"(d[c][1]), "+v"(res[pz]), "+v"(res[pz + 1])                                                       \
               : "a"(s.K[16 * sp + z]), "a"(s.K[16 * sp + z + 1]), "a"(s.K[16 * sp + 4 + z]), "a"(s.K[16 * sp + 5 + z]),                \
                 "a"(s.K[16 * sp + 8 + z]), "a"(s.K[16 * sp + 9 + z]), "a"(s.K[16 * sp + 12 + z]), "a"(s.K[16 * sp + 13 + z]),          \
                 "v"(bq[0]), "v"(bq[1]), "v"(bq[2]), "v"(bq[3]),                                                                        \
                 "v"(d[c ^ 1][0][0]), "v"(d[c ^ 1][0][1]), "v"(d[c ^ 1][0][2]), "v"(d[c ^ 1][0][3]),                                    \
                 "v"(d[c ^ 1][1][0]), "v"(d[c ^ 1][1][1]), "v"(d[c ^ 1][1][2]), "v"(d[c ^ 1][1][3]))
template <int P> DI void qk_period(St& s, const v4u (&bq)[4], f32x4 (&d)[2][2], float (&res)[4]) {
  constexpr int z = 2 * (P >> 2), sp = P & 3, c = P & 1, pz = P > 0 ? 2 * ((P - 1) >> 2) : 0, ps = (P - 1) & 3;
  if constexpr (P == 0) {
    asm volatile(MF "%0, %2, %10, 0\n\t" MF "%1, %3, %10, 0\n\t" MF "%0, %4, %11, %0\n\t" MF "%1, %5, %11, %1\n\t"
                 MF "%0, %6, %12, %0\n\t" MF "%1, %7, %12, %1\n\t" MF "%0, %8, %13, %0\n\t" MF "%1, %9, %13, %1"
                 : "=&v"(d[0][0]), "=&v"(d[0][1])
                 : "a"(s.K[0]), "a"(s.K[1]), "a"(s.K[4]), "a"(s.K[5]), "a"(s.K[8]), "a"(s.K[9]), "a"(s.K[12]), "a"(s.K[13]),
                   "v"(bq[0]), "v"(bq[1]), "v"(bq[2]), "v"(bq[3]));
  } else if constexpr (P == 8) {
    asm volatile("s_nop 5\n\t" FOLD("%0", "%1", "%2", "%6", ROT3, "8") "s_nop 0\n\t" FOLD("%0", "%1", "%3", "%7", ROT2, "8") "s_nop 0\n\t"
                 FOLD("%0", "%1", "%4", "%8", ROT1, "8") "s_nop 0\n\t" FOLD("%0", "%1", "%5", "%9", ROT0, "8")
                 : "+v"(res[2]), "+v"(res[3])
                 : "v"(d[1][0][0]), "v"(d[1][0][1]), "v"(d[1][0][2]), "v"(d[1][0][3]), "v"(d[1][1][0]), "v"(d[1][1][1]), "v"(d[1][1][2]), "v"(d[1][1][3]));
  } else if constexpr (ps == 0) QKP(ROT0, ROT3, ROT2, ROT1, "1");
  else if constexpr (ps == 1) QKP(ROT1, ROT0, ROT3, ROT2, "2");
  else if constexpr (ps == 2) QKP(ROT2, ROT1, ROT0, ROT3, "4");
  else QKP(ROT3, ROT2, ROT1, ROT0, "8");
}
template <int N> DI u32 bcast(u32 x) { return (u32)__builtin_amdgcn_mov_dpp((int)x, 0x150 + N, 0xf, 0xf, true); }

struct Pv { v4u a[2]; u32 xb; f32x4 d1, d2; v4u t[2][4]; u32 X[2], sel[4]; };
template <int J> DI void pv_step(St& s, Pv& p) {
  if constexpr ((J & 1) == 0 && J < 12) { issue_v<10 + J / 2>(s); }
  constexpr int G = (11 + J / 2) < 16 ? (11 + J / 2) : 16;
  if constexpr (J + 2 < 9) VMWAIT(4 * (G - (J + 3)));
  if constexpr (J >= 9) {
    VMWAIT(4 * (G - (J + 1)));
    asm volatile("" : "+v"(s.V[(J - 9) * 4]), "+v"(s.V[(J - 9) * 4 + 1]), "+v"(s.V[(J - 9) * 4 + 2]), "+v"(s.V[(J - 9) * 4 + 3]));
  }
  GAP();
#define VT(i) (J < 9 ? p.t[J & 1][i] : s.V[J < 9 ? 0 : (J - 9) * 4 + (i)])
#define RD(i) do { if constexpr (J + 2 < 9) p.t[J & 1][i] = lds_ld<v4u>(s.lbase + s.voff + ((J + 2) * 4 + (i)) * 1024); } while (0)
  mf_v(p.d1, p.a[0], VT(0)); GAP();
  p.a[1][1] = __builtin_amdgcn_perm(p.xb, p.xb, p.sel[1]); p.a[1][2] = __builtin_amdgcn_perm(p.xb, p.xb, p.sel[2]); p.a[1][3] = __builtin_amdgcn_perm(p.xb, p.xb, p.sel[3]);
  RD(0); GAP();
  mf_v(p.d2, p.a[0], VT(1)); GAP();
  if constexpr (J < 15) { p.xb = bcast<(J + 1) & 15>(p.X[0]); p.a[0][0] = __builtin_amdgcn_perm(p.xb, p.xb, p.sel[0]); } else asm volatile("s_nop 0");
  RD(1); GAP();
  mf_v(p.d1, p.a[1], VT(2)); GAP();
  if constexpr (J < 15) { p.a[0][1] = __builtin_amdgcn_perm(p.xb, p.xb, p.sel[1]); p.a[0][2] = __builtin_amdgcn_perm(p.xb, p.xb, p.sel[2]); p.a[0][3] = __builtin_amdgcn_perm(p.xb, p.xb, p.sel[3]); }
  RD(2); GAP();
  mf_v(p.d2, p.a[1], VT(3)); GAP();
  if constexpr (J < 15) { p.xb = bcast<(J + 1) & 15>(p.X[1]); p.a[1][0] = __builtin_amdgcn_perm(p.xb, p.xb, p.sel[0]); }
  RD(3); GAP();
#undef VT
#undef RD
}

__global__ void __launch_bounds__(256, 1)
mega(const char* x, const char* kc, const char* vc, const char* wq, const char* wo, const char* w1, const char* b1,
     const char* w2, const char* b2, char* out, char* ws) {
  extern __shared__ __attribute__((aligned(16))) char smem[];
  STAMP(0);
  const int lane = threadIdx.x & 63, w = __builtin_amdgcn_readfirstlane(threadIdx.x >> 6), g = lane >> 4, n = lane & 15;
  const int wg = blockIdx.x, xg = wg & 7, r = wg >> 3, sub = r & 3;
  const int bh = (wg & ~7) | ((wg + r) & 7), b = bh >> 5, h = bh & 31;
  const int grp = xg * 4 + ((r - 16) >> 2);
  const bool isA = r < 16, isC = !isA && grp < 20, isE = !isA && grp >= 20 && grp < 30;
  const bool isD = isA && r < (xg < 3 ? 16 : 12);
  const int aidx = xg * 16 + r, fidx = (xg < 3 ? 16 * xg : 48 + 12 * (xg - 3)) + r;
  const int kh = grp >= 10 ? 1 : 0, cs = (isC ? grp - 10 * kh : grp - 20) * 4 + sub;
  const u32 lds0 = (u32)(size_t)(LAS char*)smem;
  St s;
  s.voff = lane * 16; s.lbase = lds0 + w * LDS_WAVE;
  const size_t kvoff = (size_t)bh * 1048576 + 786432 + w * 4096;
  const char* kb = kc + kvoff;
  s.vb = vc + kvoff;
  const u32 koff = (n >> 2) * 256 + g * 64 + (n & 3) * 16;
  const int eh = lane >> 5, eb = (lane >> 2) & 7, ec = lane & 3, ecol = 8 * eh + 2 * ec;

  u32 epoch, aux;
  {
    const u32 z = 0;
    asm volatile("global_load_dword %0, %1, %2 sc1" : "=v"(epoch) : "v"(z), "s"(ws) : "memory");
    const char* ap = isD ? b1 + fidx * 32 : isE ? b2 + cs * 32 : isC ? x + cs * 32 : x;
    const u32 ao = ecol * 2 + (isC ? eb * 1280 : 0);
    asm volatile("global_load_dword %0, %1, %2" : "=v"(aux) : "v"(ao), "s"(ap) : "memory");
  }
  if (w == 0) {
    const u32 rblk = isC ? WS_XP + (cs * 2 + kh) * 256 : isD ? WS_H + fidx * 256 : isE ? WS_X2 + cs * 256 : WS_DUMMY;
    const u32 rl = isC ? XP_L : isD ? H_L : 0;
    const u32 ro = g < 2 ? WS_O + g * O_L + h * 2048 + b * 128 + (n >> 3) * 1024 + (n & 7) * 16 : rblk + (g - 2) * rl + n * 16;
    const v4u sv = {SENT, SENT, SENT, SENT};
    asm volatile("global_store_dwordx4 %0, %1, %2 sc1\n\ts_nop 1" ::"v"(ro), "v"(sv), "s"(ws) : "memory");
  }
  v4u B[5];
#pragma unroll
  for (int i = 0; i < 5; i++) B[i] = v4u{ONES, ONES, ONES, ONES};
  auto dmaW = [&](const char* base, u32 o, int cnt, int mod, int start, int step, u32 ldst) {
    int ii = start;
    for (int i = 0; i < cnt; i++) {
      const char* p = base + ii * step;
      const u32 m0v = ldst + ii * 1024;
      asm volatile("s_mov_b32 m0, %2\n\ts_nop 0\n\tglobal_load_lds_dwordx4 %0, %1 nt" ::"v"(o), "s"(p), "s"(m0v) : "memory");
      ii++; if (ii == mod) ii = 0;
    }
  };
  if (isA) {
    const u32 xo = (n & 7) * 1280 + g * 16;
    const char* xb = x + w * 320;
    u64 sv; const u64 mk = 0x00FF00FF00FF00FFull;
    asm volatile("s_mov_b64 %5, exec\n\ts_mov_b64 exec, %8\n\t"
                 "global_load_dwordx4 %0, %6, %7\n\tglobal_load_dwordx4 %1, %6, %7 offset:64\n\tglobal_load_dwordx4 %2, %6, %7 offset:128\n\t"
                 "global_load_dwordx4 %3, %6, %7 offset:192\n\tglobal_load_dwordx4 %4, %6, %7 offset:256\n\ts_mov_b64 exec, %5"
                 : "+v"(B[0]), "+v"(B[1]), "+v"(B[2]), "+v"(B[3]), "+v"(B[4]), "=&s"(sv) : "v"(xo), "s"(xb), "s"(mk) : "memory");
    dmaW(wq + (size_t)(w * 160) * 8192 + aidx * 64, (lane >> 2) * 8192 + (lane & 3) * 16, 10, 10, 5 * (r & 1), 16 * 8192, s.lbase);
    if (isD) dmaW(w1 + (size_t)(w * 160) * 3456 + fidx * 32, (lane >> 1) * 3456 + (lane & 1) * 16, 5, 5, sub, 32 * 3456, s.lbase + 10240);
  } else if (isC) {
    dmaW(wo + (size_t)(kh * 2048 + w * 512) * 1280 + cs * 32, (lane >> 1) * 1280 + (lane & 1) * 16, 16, 16, 4 * sub, 32 * 1280, s.lbase);
  } else if (isE) {
    const int cnt = w == 3 ? 12 : 14;
    dmaW(w2 + (size_t)(w * 448) * 1280 + cs * 32, (lane >> 1) * 1280 + (lane & 1) * 16, cnt, cnt, 3 * sub, 32 * 1280, s.lbase);
  }
#pragma unroll
  for (int j = 0; j < 4; j++) { const char* base = kb + j * 16384; LD4("=a", s.K, 4 * j, koff, base); }
  asm volatile("s_waitcnt vmcnt(16)" : "+v"(epoch), "+v"(aux), "+v"(B[0]), "+v"(B[1]), "+v"(B[2]), "+v"(B[3]), "+v"(B[4])::"memory");
  const u32 tag = __builtin_amdgcn_readfirstlane(epoch) + 1u;
  if (w == 0 && lane == 0) st_sc1(ws, WS_FLAGS + wg * 4, tag);
  v4u W[16];
  {
    const int q4 = (lane >> 2) & 3, p4 = lane & 3;
    if (isA) {
      const u32 a0 = s.lbase + (8 * g + q4) * 64 + 8 * p4, d0 = s.lbase + 10240 + (8 * g + q4) * 32 + 8 * p4;
#pragma unroll
      for (int i = 0; i < 10; i++) W[i] = tr_frag(a0 + (i >> 1) * 2048 + (i & 1) * 32, 256);
#pragma unroll
      for (int i = 0; i < 5; i++) W[10 + i] = tr_frag(d0 + i * 1024, 128);
      W[15] = v4u{ONES, ONES, ONES, ONES};
    } else {
      const u32 c0 = s.lbase + (32 * (p4 >> 1) + 8 * g + q4) * 32 + 8 * (p4 & 1);
#pragma unroll
      for (int i = 0; i < 16; i++) W[i] = tr_frag(c0 + (i >> 1) * 2048 + (i & 1) * 16, 128);
    }
  }
  asm volatile("s_waitcnt lgkmcnt(0)" ::: "memory");
  STAMP(1);
  f32x2 csD = {0.f, 0.f};
  float res_lo = bf_lo(aux), res_hi = bf_hi(aux);
  float xr_lo = 0.f, xr_hi = 0.f;

#pragma unroll 1
  for (int l = 0; l < 2; l++) {
    const bool first = l == 0;
    OPAQUE(ws);
    if (isA) {
      LANES();
      if (!first) {
#pragma unroll
        for (int i = 0; i < 5; i++) B[i] = v4u{ONES, ONES, ONES, ONES};
        const u32 xo = g * 128 + (n & 7) * 16;
        const char* xb = ws + WS_X2 + w * 2560;
        for (int it = 0; it < SPIN_CAP; it++) {
          u64 sv; const u64 mk = 0x00FF00FF00FF00FFull;
          asm volatile("s_mov_b64 %5, exec\n\ts_mov_b64 exec, %8\n\t"
                       "global_load_dwordx4 %0, %6, %7 sc1\n\tglobal_load_dwordx4 %1, %6, %7 offset:512 sc1\n\tglobal_load_dwordx4 %2, %6, %7 offset:1024 sc1\n\t"
                       "global_load_dwordx4 %3, %6, %7 offset:1536 sc1\n\tglobal_load_dwordx4 %4, %6, %7 offset:2048 sc1\n\ts_mov_b64 exec, %5\n\ts_waitcnt vmcnt(0)"
                       : "+v"(B[0]), "+v"(B[1]), "+v"(B[2]), "+v"(B[3]), "+v"(B[4]), "=&s"(sv) : "v"(xo), "s"(xb), "s"(mk) : "memory");
          const u32 mx = max(max(max(umax4(B[0]), umax4(B[1])), max(umax4(B[2]), umax4(B[3]))), umax4(B[4]));
          if (!__any(mx == SENT)) break;
        }
        STAMP(9);
      }
      f32x4 t0, t1, gr;
      asm volatile("s_nop 1\n\t" MF "%0, %3, %13, 0\n\t" MF "%1, %4, %13, 0\n\t" MF "%2, %13, %13, 0\n\t"
                   MF "%0, %5, %14, %0\n\t" MF "%1, %6, %14, %1\n\t" MF "%2, %14, %14, %2\n\t"
                   MF "%0, %7, %15, %0\n\t" MF "%1, %8, %15, %1\n\t" MF "%2, %15, %15, %2\n\t"
                   MF "%0, %9, %16, %0\n\t" MF "%1, %10, %16, %1\n\t" MF "%2, %16, %16, %2\n\t"
                   MF "%0, %11, %17, %0\n\t" MF "%1, %12, %17, %1\n\t" MF "%2, %17, %17, %2\n\ts_nop 7"
                   : "=&v"(t0), "=&v"(t1), "=&v"(gr)
                   : "v"(W[0]), "v"(W[1]), "v"(W[2]), "v"(W[3]), "v"(W[4]), "v"(W[5]), "v"(W[6]), "v"(W[7]), "v"(W[8]), "v"(W[9]),
                     "v"(B[0]), "v"(B[1]), "v"(B[2]), "v"(B[3]), "v"(B[4]));
      if (n <= 8) {
        const u32 a = n < 8 ? lds0 + SC_ROLE + w * 1024 + (g >> 1) * 256 + n * 32 + (g & 1) * 16 : lds0 + SC_CS + w * 128 + g * 16;
        lds_st<f32x4>(a, t0);
        lds_st<f32x4>(a + (n < 8 ? 512 : 64), t1);
      }
      if (n < 8 && (g == 2 || g == (n >> 2))) {
        const float gv = g == 2 ? gr[0] : (n & 3) == 0 ? gr[0] : (n & 3) == 1 ? gr[1] : (n & 3) == 2 ? gr[2] : gr[3];
        lds_st<float>(lds0 + SC_STAT + w * 64 + n * 8 + (g == 2 ? 4 : 0), gv);
      }
      if (first) {
        f32x4 c1;
        asm volatile("s_nop 1\n\t" MF "%0, %1, %6, 0\n\t" MF "%0, %2, %6, %0\n\t" MF "%0, %3, %6, %0\n\t" MF "%0, %4, %6, %0\n\t" MF "%0, %5, %6, %0\n\ts_nop 7"
                     : "=&v"(c1) : "v"(W[10]), "v"(W[11]), "v"(W[12]), "v"(W[13]), "v"(W[14]), "v"(W[15]));
        if (n == 0) lds_st<f32x4>(lds0 + SC_CS + 512 + w * 64 + g * 16, c1);
      }
      lds_barrier();
      if (w < 2) {
        f32x2 acc = {0.f, 0.f}, csum = {0.f, 0.f}, st = {0.f, 0.f};
#pragma unroll
        for (int ww = 0; ww < 4; ww++) {
          acc += lds_ld<f32x2>(lds0 + SC_ROLE + ww * 1024 + w * 512 + lane * 8);
          csum += lds_ld<f32x2>(lds0 + SC_CS + ww * 128 + w * 64 + ecol * 4);
          st += lds_ld<f32x2>(lds0 + SC_STAT + ww * 64 + eb * 8);
        }
        if (first) {
#pragma unroll
          for (int ww = 0; ww < 4; ww++) csD += lds_ld<f32x2>(lds0 + SC_CS + 512 + ww * 64 + ecol * 4);
        }
        const float mean = st[1] * (1.f / 640.f), var = fmaxf(st[0] * (1.f / 640.f) - mean * mean, 0.f);
        const float rs = __builtin_amdgcn_rsqf(var + 1e-5f);
        const v2u gq = {pk_bf16(rs * (acc[0] - mean * csum[0]), rs * (acc[1] - mean * csum[1])), tag};
        st2_sc1(ws + WS_Q + l * Q_L, (eb * 128 + aidx) * 128 + (8 * w + 4 * eh + ec) * 8, gq);
      }
      if (!first) STAMP(10);
    }
    LANES();
    s.voff = lane * 16; s.vrun = s.voff; s.skip = l;
    OPAQUE(s.vb); OPAQUE(s.lbase);
    const u32 qoff = (4 * g + (n >> 2)) * 32;
    const char* qb = ws + WS_Q + l * Q_L + (b * 128 + h * 4) * 128;
    v4u qg0, qg1;
    bool ok = false;
    if (first) {
      const u32 koff2 = (n >> 2) * 256 + g * 64 + (n & 3) * 16;
      OPAQUE(kb);
#pragma unroll
      for (int j = 4; j < 16; j++) { const char* base = kb + j * 16384; LD4("+a", s.K, 4 * j, koff2, base); }
      v4u fl;
      const char* fb = ws + WS_FLAGS;
      asm volatile("global_load_dwordx4 %0, %3, %4 sc1\n\tglobal_load_dwordx4 %1, %3, %4 offset:16 sc1\n\tglobal_load_dwordx4 %2, %5, %6 sc1"
                   : "=&v"(qg0), "=&v"(qg1), "=&v"(fl) : "v"(qoff), "s"(qb), "v"(s.voff), "s"(fb) : "memory");
      issue_v<0>(s); issue_v<1>(s);
      asm volatile("s_waitcnt vmcnt(8)" : "+v"(qg0), "+v"(qg1), "+v"(fl)::"memory");
#pragma unroll 1
      for (int it = 0; it < SPIN_CAP; it++) {
        if (__all(fl[0] == tag && fl[1] == tag && fl[2] == tag && fl[3] == tag)) break;
        asm volatile("global_load_dwordx4 %0, %1, %2 sc1\n\ts_waitcnt vmcnt(0)" : "=&v"(fl) : "v"(s.voff), "s"(fb) : "memory");
      }
      ok = __all(qg0[1] == tag && qg0[3] == tag && qg1[1] == tag && qg1[3] == tag);
    } else asm volatile("" : "=v"(qg0), "=v"(qg1));
#pragma unroll 1
    for (int it = 0; !ok && it < SPIN_CAP; it++) {
      asm volatile("global_load_dwordx4 %0, %2, %3 sc1\n\tglobal_load_dwordx4 %1, %2, %3 offset:16 sc1\n\ts_waitcnt vmcnt(0)"
                   : "=&v"(qg0), "=&v"(qg1) : "v"(qoff), "s"(qb) : "memory");
      ok = __all(qg0[1] == tag && qg0[3] == tag && qg1[1] == tag && qg1[3] == tag);
    }
    if (first) STAMP(2); else STAMP(11);
    float res[4] = {0.f, 0.f, 0.f, 0.f};
    {
      v4u bq[4];
#pragma unroll
      for (int u = 0; u < 4; u++) { const bool on = (n & 3) == u; bq[u] = v4u{on ? qg0[0] : 0u, on ? qg0[2] : 0u, on ? qg1[0] : 0u, on ? qg1[2] : 0u}; }
      asm volatile("s_nop 1" : "+v"(bq[0]), "+v"(bq[1]), "+v"(bq[2]), "+v"(bq[3]), "+v"(res[0]), "+v"(res[1]), "+v"(res[2]), "+v"(res[3]));
      f32x4 d[2][2];
      issue_v<2>(s);
      qk_period<0>(s, bq, d, res); issue_v<3>(s);
      qk_period<1>(s, bq, d, res); issue_v<4>(s);
      qk_period<2>(s, bq, d, res); issue_v<5>(s);
      qk_period<3>(s, bq, d, res); issue_v<6>(s);
      qk_period<4>(s, bq, d, res); issue_v<7>(s);
      qk_period<5>(s, bq, d, res); issue_v<8>(s);
      qk_period<6>(s, bq, d, res); issue_v<9>(s);
      qk_period<7>(s, bq, d, res);
      qk_period<8>(s, bq, d, res);
    }
    Pv p;
    VMWAIT(32);
#pragma unroll
    for (int i = 0; i < 8; i++) p.t[i >> 2][i & 3] = lds_ld<v4u>(s.lbase + s.voff + i * 1024);
    constexpr float C2 = 0.08838834764831845f * 1.4426950408889634f;
    const float mw = wave_max(fmaxf(fmaxf(res[0], res[1]), fmaxf(res[2], res[3]))) * C2;
    float pr[4];
#pragma unroll
    for (int z = 0; z < 4; z++) pr[z] = __builtin_amdgcn_exp2f(__builtin_fmaf(res[z], C2, -mw));
    p.X[0] = pk_bf16(pr[0], pr[1]); p.X[1] = pk_bf16(pr[2], pr[3]);
    const float lw = wave_sum((pr[0] + pr[1]) + (pr[2] + pr[3]));
#pragma unroll
    for (int rr = 0; rr < 4; rr++) {
      const u32 src = n < 8 ? 0x0100u : 0x0302u;
      p.sel[rr] = ((n & 7) >> 1) != rr ? 0x0c0c0c0cu : (n & 1) ? (src << 16) | 0x0c0cu : 0x0c0c0000u | src;
    }
    p.d1 = f32x4{0.f, 0.f, 0.f, 0.f}; p.d2 = p.d1;
    p.xb = bcast<0>(p.X[0]);
#pragma unroll
    for (int rr = 0; rr < 4; rr++) p.a[0][rr] = __builtin_amdgcn_perm(p.xb, p.xb, p.sel[rr]);
    p.xb = bcast<0>(p.X[1]);
    p.a[1][0] = __builtin_amdgcn_perm(p.xb, p.xb, p.sel[0]);
    asm volatile("s_nop 1" : "+v"(p.a[0]), "+v"(p.d1), "+v"(p.d2));
    GAP();
    pv_step<0>(s, p); pv_step<1>(s, p); pv_step<2>(s, p); pv_step<3>(s, p);
    pv_step<4>(s, p); pv_step<5>(s, p); pv_step<6>(s, p); pv_step<7>(s, p);
    pv_step<8>(s, p); pv_step<9>(s, p); pv_step<10>(s, p); pv_step<11>(s, p);
    pv_step<12>(s, p); pv_step<13>(s, p); pv_step<14>(s, p); pv_step<15>(s, p);
    asm volatile("s_nop 7" : "+v"(p.d1), "+v"(p.d2));
    if (first) STAMP(3); else STAMP(12);
    {
    LANES();
    lds_st<f32x4>(lds0 + SC_ATT + (w * 2 + (g >> 1)) * 512 + n * 32 + (g & 1) * 16, g < 2 ? p.d1 : p.d2);
    if (lane == 0) { lds_st<float>(lds0 + SC_ML + w * 4, mw); lds_st<float>(lds0 + SC_ML + 16 + w * 4, lw); }
    lds_barrier();
    if (w == 0) {
      const f32x4 m4 = lds_ld<f32x4>(lds0 + SC_ML), l4 = lds_ld<f32x4>(lds0 + SC_ML + 16);
      const float M = fmaxf(fmaxf(m4[0], m4[1]), fmaxf(m4[2], m4[3]));
      f32x2 o = {0.f, 0.f}; float L = 0.f;
#pragma unroll
      for (int ww = 0; ww < 4; ww++) {
        const float f = __builtin_amdgcn_exp2f(m4[ww] - M);
        L += f * l4[ww];
        o += f * (lds_ld<f32x2>(lds0 + SC_ATT + ww * 1024 + lane * 8) + lds_ld<f32x2>(lds0 + SC_ATT + ww * 1024 + 512 + lane * 8));
      }
      const float inv = __builtin_amdgcn_rcpf(L);
      st_sc1(ws + WS_O + l * O_L + h * 2048 + b * 128, eh * 1024 + (lane & 31) * 4, min(pk_bf16(o[0] * inv, o[1] * inv), SENT - 1));
    }
    }
    if (first) STAMP(4); else STAMP(13);
    if (isC || isE || isD) {
      LANES();
      OPAQUE(ws);
      if (isD) {
        v4u B[5];
        f32x4 t0, t1, t2;
        const char* xb = ws + WS_XP + l * XP_L + w * 5120 + 2048;
        const u32 xo = (g >> 1) * 512 + (n >> 3) * 256 + (g & 1) * 128 + (n & 7) * 16;
        for (int it = 0; it < SPIN_CAP; it++) {
          asm volatile("global_load_dwordx4 %0, %5, %6 offset:-2048 sc1\n\tglobal_load_dwordx4 %1, %5, %6 offset:-1024 sc1\n\t"
                       "global_load_dwordx4 %2, %5, %6 sc1\n\tglobal_load_dwordx4 %3, %5, %6 offset:1024 sc1\n\t"
                       "global_load_dwordx4 %4, %5, %6 offset:2048 sc1\n\ts_waitcnt vmcnt(0)"
                       : "=&v"(B[0]), "=&v"(B[1]), "=&v"(B[2]), "=&v"(B[3]), "=&v"(B[4]) : "v"(xo), "s"(xb) : "memory");
          const u32 mx = max(max(max(umax4(B[0]), umax4(B[1])), max(umax4(B[2]), umax4(B[3]))), umax4(B[4]));
          if (!__any(mx == SENT)) break;
        }
        if (first) STAMP(5); else STAMP(14);
        asm volatile("s_nop 1\n\t" MF "%0, %3, %9, 0\n\t" MF "%1, %9, %9, 0\n\t" MF "%2, %8, %9, 0\n\t"
                     MF "%0, %4, %10, %0\n\t" MF "%1, %10, %10, %1\n\t" MF "%2, %8, %10, %2\n\t"
                     MF "%0, %5, %11, %0\n\t" MF "%1, %11, %11, %1\n\t" MF "%2, %8, %11, %2\n\t"
                     MF "%0, %6, %12, %0\n\t" MF "%1, %12, %12, %1\n\t" MF "%2, %8, %12, %2\n\t"
                     MF "%0, %7, %13, %0\n\t" MF "%1, %13, %13, %1\n\t" MF "%2, %8, %13, %2\n\ts_nop 7"
                     : "=&v"(t0), "=&v"(t1), "=&v"(t2)
                     : "v"(W[10]), "v"(W[11]), "v"(W[12]), "v"(W[13]), "v"(W[14]), "v"(W[15]), "v"(B[0]), "v"(B[1]), "v"(B[2]), "v"(B[3]), "v"(B[4]));
        lds_st<f32x4>(lds0 + SC_ROLE + (w * 2 + (n >> 3)) * 512 + (g >> 1) * 256 + (n & 7) * 32 + (g & 1) * 16, t0);
        const bool isG = (g & 1) == ((n >> 2) & 1);
        const float gv = !isG ? t2[0] : (n & 3) == 0 ? t1[0] : (n & 3) == 1 ? t1[1] : (n & 3) == 2 ? t1[2] : t1[3];
        lds_st<float>(lds0 + SC_STAT + w * 256 + (n & 7) * 32 + (isG ? 0 : 16) + (g >> 1) * 8 + (n >> 3) * 4, gv);
      } else {
        if (isE && w == 0) {
          const char* xb = ws + WS_XP + l * XP_L + cs * 512;
          const u32 xo = lane * 4;
          u32 p1, p2;
          for (int it = 0; it < SPIN_CAP; it++) {
            asm volatile("global_load_dword %0, %2, %3 sc1\n\tglobal_load_dword %1, %2, %3 offset:256 sc1\n\ts_waitcnt vmcnt(0)"
                         : "=&v"(p1), "=&v"(p2) : "v"(xo), "s"(xb) : "memory");
            if (!__any(max(p1, p2) == SENT)) break;
          }
          xr_lo = bf_lo(p1) + bf_lo(p2); xr_hi = bf_hi(p1) + bf_hi(p2);
        }
        v4u B[8];
        f32x4 t0, t1;
        B[6] = v4u{0u, 0u, 0u, 0u}; B[7] = B[6];
        const int extra = __builtin_amdgcn_readfirstlane(isC ? 2 : (w == 3 ? 0 : 1));
        const char* gb = ws + (isC ? WS_O + l * O_L + (kh * 16 + 4 * w) * 2048 : WS_H + l * H_L + w * 7168) + 4096;
        const u32 go = isC ? (n & 7) * 128 + (n >> 3) * 64 + g * 16 : (n >> 3) * 512 + g * 128 + (n & 7) * 16;
        for (int it = 0; it < SPIN_CAP; it++) {
          asm volatile("global_load_dwordx4 %0, %8, %9 offset:-4096 sc1\n\tglobal_load_dwordx4 %1, %8, %9 offset:-3072 sc1\n\t"
                       "global_load_dwordx4 %2, %8, %9 offset:-2048 sc1\n\tglobal_load_dwordx4 %3, %8, %9 offset:-1024 sc1\n\t"
                       "global_load_dwordx4 %4, %8, %9 sc1\n\tglobal_load_dwordx4 %5, %8, %9 offset:1024 sc1\n\t"
                       "s_cmp_lt_i32 %10, 1\n\ts_cbranch_scc1 .Ly%=\n\tglobal_load_dwordx4 %6, %8, %9 offset:2048 sc1\n\t"
                       "s_cmp_lt_i32 %10, 2\n\ts_cbranch_scc1 .Ly%=\n\tglobal_load_dwordx4 %7, %8, %9 offset:3072 sc1\n.Ly%=:\n\ts_waitcnt vmcnt(0)"
                       : "=&v"(B[0]), "=&v"(B[1]), "=&v"(B[2]), "=&v"(B[3]), "=&v"(B[4]), "=&v"(B[5]), "+v"(B[6]), "+v"(B[7])
                       : "v"(go), "s"(gb), "s"(extra) : "memory", "scc");
          const u32 mx = max(max(max(umax4(B[0]), umax4(B[1])), max(umax4(B[2]), umax4(B[3]))), max(max(umax4(B[4]), umax4(B[5])), max(umax4(B[6]), umax4(B[7]))));
          if (!__any(mx == SENT)) break;
        }
        if (first) STAMP(5); else STAMP(14);
        asm volatile("s_nop 1\n\t" MF "%0, %2, %18, 0\n\t" MF "%1, %3, %18, 0\n\t" MF "%0, %4, %19, %0\n\t" MF "%1, %5, %19, %1\n\t"
                     MF "%0, %6, %20, %0\n\t" MF "%1, %7, %20, %1\n\t" MF "%0, %8, %21, %0\n\t" MF "%1, %9, %21, %1\n\t"
                     MF "%0, %10, %22, %0\n\t" MF "%1, %11, %22, %1\n\t" MF "%0, %12, %23, %0\n\t" MF "%1, %13, %23, %1\n\t"
                     "s_cmp_lt_i32 %26, 1\n\ts_cbranch_scc1 .Lx%=\n\t" MF "%0, %14, %24, %0\n\t" MF "%1, %15, %24, %1\n\t"
                     "s_cmp_lt_i32 %26, 2\n\ts_cbranch_scc1 .Lx%=\n\t" MF "%0, %16, %25, %0\n\t" MF "%1, %17, %25, %1\n.Lx%=:\n\ts_nop 7"
                     : "=&v"(t0), "=&v"(t1)
                     : "v"(W[0]), "v"(W[1]), "v"(W[2]), "v"(W[3]), "v"(W[4]), "v"(W[5]), "v"(W[6]), "v"(W[7]), "v"(W[8]), "v"(W[9]), "v"(W[10]), "v"(W[11]),
                       "v"(W[12]), "v"(W[13]), "v"(W[14]), "v"(W[15]),
                       "v"(B[0]), "v"(B[1]), "v"(B[2]), "v"(B[3]), "v"(B[4]), "v"(B[5]), "v"(B[6]), "v"(B[7]), "s"(extra) : "scc");
        if ((g >> 1) == (n >> 3)) {
          const u32 a = lds0 + SC_ROLE + (w * 2 + (n >> 3)) * 512 + (n & 7) * 32 + (g & 1) * 16;
          lds_st<f32x4>(a, t0);
          lds_st<f32x4>(a + 256, t1);
        }
      }
      lds_barrier();
      if (w == 0) {
        f32x2 acc = {0.f, 0.f};
#pragma unroll
        for (int i = 0; i < 8; i++) acc += lds_ld<f32x2>(lds0 + SC_ROLE + i * 512 + lane * 8);
        if (isD) {
          float ss = 0.f, sm = 0.f;
#pragma unroll
          for (int ww = 0; ww < 4; ww++) {
            const f32x4 gq = lds_ld<f32x4>(lds0 + SC_STAT + ww * 256 + eb * 32);
            const f32x2 rq = lds_ld<f32x2>(lds0 + SC_STAT + ww * 256 + eb * 32 + 16);
            ss += (gq[0] + gq[1]) + (gq[2] + gq[3]); sm += rq[0] + rq[1];
          }
          const float mean = sm * (1.f / 640.f), var = fmaxf(ss * (1.f / 640.f) - mean * mean, 0.f);
          const float r1 = __builtin_amdgcn_rsqf(var + 1e-5f);
          const float rs = r1 * __builtin_amdgcn_rsqf(var * r1 * r1 + 1e-5f);
          const float y0 = rs * (acc[0] - mean * csD[0]) + res_lo, y1 = rs * (acc[1] - mean * csD[1]) + res_hi;
          constexpr float GC = -2.f * 0.7978845608028654f * 1.4426950408889634f;
          const float e0 = __builtin_amdgcn_exp2f(GC * (y0 + 0.044715f * y0 * y0 * y0)), e1 = __builtin_amdgcn_exp2f(GC * (y1 + 0.044715f * y1 * y1 * y1));
          const u32 hv = pk_bf16(y0 * __builtin_amdgcn_rcpf(1.f + e0), y1 * __builtin_amdgcn_rcpf(1.f + e1));
          st_sc1(ws + WS_H + l * H_L + fidx * 256, lane * 4, min(hv, SENT - 1));
        } else if (isC) {
          const u32 xv = kh ? pk_bf16(acc[0], acc[1]) : pk_bf16(acc[0] + res_lo, acc[1] + res_hi);
          st_sc1(ws + WS_XP + l * XP_L + (cs * 2 + kh) * 256, lane * 4, min(xv, SENT - 1));
          if (first && kh == 0) {
            const char* xb = ws + WS_X2 + cs * 256;
            const u32 xo = lane * 4;
            u32 p1;
            for (int it = 0; it < SPIN_CAP; it++) {
              asm volatile("global_load_dword %0, %1, %2 sc1\n\ts_waitcnt vmcnt(0)" : "=&v"(p1) : "v"(xo), "s"(xb) : "memory");
              if (!__any(p1 == SENT)) break;
            }
            res_lo = bf_lo(p1); res_hi = bf_hi(p1);
          }
        } else {
          const u32 xv = pk_bf16(xr_lo + acc[0] + res_lo, xr_hi + acc[1] + res_hi);
          if (first) st_sc1(ws + WS_X2 + cs * 256, lane * 4, min(xv, SENT - 1));
          else {
            *(GAS u32*)(out + eb * 1280 + cs * 32 + ecol * 2) = xv;
            if (cs == 0 && lane == 0) st_sc1(ws, WS_EPOCH, tag);
          }
        }
      }
      if (first) STAMP(6); else STAMP(15);
    }
    asm volatile("" : "=v"(B[0]), "=v"(B[1]), "=v"(B[2]), "=v"(B[3]), "=v"(B[4]));
  }
}

extern "C" void kernel_launch(void* const* d_in, const int* in_sizes, int n_in, void* d_out, int out_size, void* d_ws,
                              size_t ws_size, hipStream_t stream) {
  (void)hipFuncSetAttribute((const void*)mega, hipFuncAttributeMaxDynamicSharedMemorySize, LDS_BYTES);
  mega<<<256, 256, LDS_BYTES, stream>>>((const char*)d_in[0], (const char*)d_in[1], (const char*)d_in[2], (const char*)d_in[3],
                                        (const char*)d_in[4], (const char*)d_in[6], (const char*)d_in[7], (const char*)d_in[8],
                                        (const char*)d_in[9], (char*)d_out, (char*)d_ws);
}
```

```cpp
#define WAIT_AT 0
#define WAIT_US 0
#include <hip/hip_runtime.h>
#include <stdint.h>

typedef unsigned u32;
typedef unsigned long long u64;
typedef u32 v4u __attribute__((ext_vector_type(4)));
typedef u32 v2u __attribute__((ext_vector_type(2)));
typedef float f32x4 __attribute__((ext_vector_type(4)));
typedef float f32x2 __attribute__((ext_vector_type(2)));
typedef short v4s __attribute__((ext_vector_type(4)));
typedef __bf16 bf16x2 __attribute__((ext_vector_type(2)));
#define LAS __attribute__((address_space(3)))
#define GAS __attribute__((address_space(1)))
#define DI __device__ __forceinline__
#define GAP() __builtin_amdgcn_sched_barrier(0)

constexpr u32 WS_EPOCH = 0, WS_FLAGS = 0x400, WS_DUMMY = 0x800;
constexpr u32 WS_Q = 0x1000, Q_L = 131072;
constexpr u32 WS_O = WS_Q + 2 * Q_L, O_L = 65536;
constexpr u32 WS_XP = WS_O + 2 * O_L, XP_L = 20480;
constexpr u32 WS_H = WS_XP + 2 * XP_L, H_L = 27648;
constexpr u32 WS_X2 = WS_H + 2 * H_L;
constexpr u32 WS_STAMP = 1u << 20;
constexpr u32 LDS_WAVE = 36864, LDS_SC = 4 * LDS_WAVE, SC_ATT = LDS_SC, SC_ML = SC_ATT + 4096, SC_ROLE = SC_ML + 256,
              SC_STAT = SC_ROLE + 4096, SC_CS = SC_STAT + 1024, LDS_BYTES = 163840;
constexpr u32 SENT = 0xFFFFFFFFu, ONES = 0x3F803F80u;
constexpr int SPIN_CAP = 1 << 17;

DI u32 pk_bf16(float lo, float hi) {
  f32x2 v = {lo, hi};
  return __builtin_bit_cast(u32, __builtin_convertvector(v, bf16x2));
}
DI float bf_lo(u32 x) { return __builtin_bit_cast(float, x << 16); }
DI float bf_hi(u32 x) { return __builtin_bit_cast(float, x & 0xffff0000u); }
DI u32 umax4(v4u a) { return max(max(a[0], a[1]), max(a[2], a[3])); }
template <int CTRL> DI float dppf(float x) {
  return __builtin_bit_cast(float, __builtin_amdgcn_mov_dpp(__builtin_bit_cast(int, x), CTRL, 0xf, 0xf, true));
}
DI float wave_max(float x) {
  x = fmaxf(x, dppf<0xB1>(x)); x = fmaxf(x, dppf<0x4E>(x)); x = fmaxf(x, dppf<0x124>(x)); x = fmaxf(x, dppf<0x128>(x));
  auto s = __builtin_amdgcn_permlane16_swap(__float_as_uint(x), __float_as_uint(x), false, false);
  x = fmaxf(__uint_as_float(s[0]), __uint_as_float(s[1]));
  auto t = __builtin_amdgcn_permlane32_swap(__float_as_uint(x), __float_as_uint(x), false, false);
  return fmaxf(__uint_as_float(t[0]), __uint_as_float(t[1]));
}
DI float wave_sum(float x) {
  x += dppf<0xB1>(x); x += dppf<0x4E>(x); x += dppf<0x124>(x); x += dppf<0x128>(x);
  auto s = __builtin_amdgcn_permlane16_swap(__float_as_uint(x), __float_as_uint(x), false, false);
  x = __uint_as_float(s[0]) + __uint_as_float(s[1]);
  auto t = __builtin_amdgcn_permlane32_swap(__float_as_uint(x), __float_as_uint(x), false, false);
  return __uint_as_float(t[0]) + __uint_as_float(t[1]);
}
DI void lds_barrier() { asm volatile("s_waitcnt lgkmcnt(0)\n\ts_barrier" ::: "memory"); }
DI void st_sc1(char* base, u32 off, u32 v) { asm volatile("global_store_dword %0, %1, %2 sc1" ::"v"(off), "v"(v), "s"(base) : "memory"); }
DI void st2_sc1(char* base, u32 off, v2u v) { asm volatile("global_store_dwordx2 %0, %1, %2 sc1" ::"v"(off), "v"(v), "s"(base) : "memory"); }
template <typename T> DI T lds_ld(u32 a) { return *(const LAS T*)(size_t)a; }
template <typename T> DI void lds_st(u32 a, T v) { *(LAS T*)(size_t)a = v; }
DI v4u tr_frag(u32 a, u32 second) {
  const v4s lo = __builtin_amdgcn_ds_read_tr16_b64_v4i16((LAS v4s*)(size_t)a);
  const v4s hi = __builtin_amdgcn_ds_read_tr16_b64_v4i16((LAS v4s*)(size_t)(a + second));
  const v2u l = __builtin_bit_cast(v2u, lo), h = __builtin_bit_cast(v2u, hi);
  return v4u{l[0], l[1], h[0], h[1]};
}
DI int lane_id() { int l; asm volatile("v_mbcnt_lo_u32_b32 %0, -1, 0\n\tv_mbcnt_hi_u32_b32 %0, -1, %0" : "=v"(l)); return l; }
#define LANES() const int lane = lane_id(), g = lane >> 4, n = lane & 15, eh = lane >> 5, eb = (lane >> 2) & 7, ec = lane & 3, ecol = 8 * eh + 2 * ec; (void)g; (void)n; (void)eb; (void)ecol
#define OPAQUE(p) asm volatile("" : "+s"(p))
#define MF "v_mfma_f32_16x16x32_bf16 "
#define VMWAIT(n) asm volatile("s_waitcnt vmcnt(%0)" ::"i"(n) : "memory")

#define LD4(C, A, i0, vo, base)                                                                                     \
  asm volatile("global_load_dwordx4 %0, %4, %5 nt\n\tglobal_load_dwordx4 %1, %4, %5 offset:1024 nt\n\t"               \
               "global_load_dwordx4 %2, %4, %5 offset:2048 nt\n\tglobal_load_dwordx4 %3, %4, %5 offset:3072 nt"       \
               : C(A[i0]), C(A[i0 + 1]), C(A[i0 + 2]), C(A[i0 + 3])                                                   \
               : "v"(vo), "s"(base)                                                                                   \
               : "memory")
#define DMA4(vo, base, m0v)                                                                                          \
  asm volatile("s_mov_b32 m0, %2\n\ts_nop 0\n\tglobal_load_lds_dwordx4 %0, %1 nt\n\t"                                  \
               "global_load_lds_dwordx4 %0, %1 offset:1024 nt\n\tglobal_load_lds_dwordx4 %0, %1 offset:2048 nt\n\t"    \
               "global_load_lds_dwordx4 %0, %1 offset:3072 nt" ::"v"(vo), "s"(base), "s"(m0v)                          \
               : "memory")

#define STAMP(i) do { } while (0)

struct St {
  v4u K[64], V[28];
  const char* vb;
  u32 voff, vrun, lbase, skip;
};
template <int J> DI void issue_v(St& s) {
  if constexpr (J < 9)
    asm volatile("s_cmp_lg_u32 %3, 0\n\ts_cbranch_scc1 .Lv%=\n\ts_add_u32 m0, %2, %4\n\ts_nop 0\n\tglobal_load_lds_dwordx4 %1, %0 nt\n\t"
                 "global_load_lds_dwordx4 %1, %0 offset:1024 nt\n\tglobal_load_lds_dwordx4 %1, %0 offset:2048 nt\n\t"
                 "global_load_lds_dwordx4 %1, %0 offset:3072 nt\n\tv_add_u32 %1, 0x4000, %1\n.Lv%=:"
                 : "+s"(s.vb), "+v"(s.vrun) : "s"(s.lbase), "s"(s.skip), "i"(J * 4096) : "memory", "scc");
  else
    asm volatile("s_cmp_lg_u32 %6, 0\n\ts_cbranch_scc1 .Lv%=\n\tglobal_load_dwordx4 %0, %5, %4 nt\n\tglobal_load_dwordx4 %1, %5, %4 offset:1024 nt\n\t"
                 "global_load_dwordx4 %2, %5, %4 offset:2048 nt\n\tglobal_load_dwordx4 %3, %5, %4 offset:3072 nt\n\tv_add_u32 %5, 0x4000, %5\n.Lv%=:"
                 : "+v"(s.V[(J - 9) * 4]), "+v"(s.V[(J - 9) * 4 + 1]), "+v"(s.V[(J - 9) * 4 + 2]), "+v"(s.V[(J - 9) * 4 + 3]), "+s"(s.vb), "+v"(s.vrun)
                 : "s"(s.skip) : "memory", "scc");
}
DI void mf_v(f32x4& d, const v4u& a, const v4u& b) { asm volatile(MF "%0, %1, %2, %0" : "+v"(d) : "v"(a), "v"(b)); }

#define ROT0 "quad_perm:[0,1,2,3]"
#define ROT1 "row_ror:4"
#define ROT2 "row_ror:8"
#define ROT3 "row_ror:12"
#define FOLD(ra, rb, da, db, rot, bank) "v_add_f32_dpp " ra ", " da ", " ra " " rot " row_mask:0xf bank_mask:" bank "\n\tv_add_f32_dpp " rb ", " db ", " rb " " rot " row_mask:0xf bank_mask:" bank "\n\t"
#define QKP(r0, r1, r2, r3, bank)                                                                                                     \
  asm volatile(MF "%0, %4, %12, 0\n\t" MF "%1, %5, %12, 0\n\t" MF "%0, %6, %13, %0\n\t" MF "%1, %7, %13, %1\n\t" MF "%0, %8, %14, %0\n\t" \
               FOLD("%2", "%3", "%16", "%20", r0, bank) MF "%1, %9, %14, %1\n\t" FOLD("%2", "%3", "%17", "%21", r1, bank)              \
               MF "%0, %10, %15, %0\n\t" FOLD("%2", "%3", "%18", "%22", r2, bank) MF "%1, %11, %15, %1\n\t" FOLD("%2", "%3", "%19", "%23", r3, bank) \
               : "=&v"(d[c][0]), "=&v"(d[c][1]), "+v"(res[pz]), "+v"(res[pz + 1])                                                       \
               : "a"(s.K[16 * sp + z]), "a"(s.K[16 * sp + z + 1]), "a"(s.K[16 * sp + 4 + z]), "a"(s.K[16 * sp + 5 + z]),                \
                 "a"(s.K[16 * sp + 8 + z]), "a"(s.K[16 * sp + 9 + z]), "a"(s.K[16 * sp + 12 + z]), "a"(s.K[16 * sp + 13 + z]),          \
                 "v"(bq[0]), "v"(bq[1]), "v"(bq[2]), "v"(bq[3]),                                                                        \
                 "v"(d[c ^ 1][0][0]), "v"(d[c ^ 1][0][1]), "v"(d[c ^ 1][0][2]), "v"(d[c ^ 1][0][3]),                                    \
                 "v"(d[c ^ 1][1][0]), "v"(d[c ^ 1][1][1]), "v"(d[c ^ 1][1][2]), "v"(d[c ^ 1][1][3]))
template <int P> DI void qk_period(St& s, const v4u (&bq)[4], f32x4 (&d)[2][2], float (&res)[4]) {
  constexpr int z = 2 * (P >> 2), sp = P & 3, c = P & 1, pz = P > 0 ? 2 * ((P - 1) >> 2) : 0, ps = (P - 1) & 3;
  if constexpr (P == 0) {
    asm volatile(MF "%0, %2, %10, 0\n\t" MF "%1, %3, %10, 0\n\t" MF "%0, %4, %11, %0\n\t" MF "%1, %5, %11, %1\n\t"
                 MF "%0, %6, %12, %0\n\t" MF "%1, %7, %12, %1\n\t" MF "%0, %8, %13, %0\n\t" MF "%1, %9, %13, %1"
                 : "=&v"(d[0][0]), "=&v"(d[0][1])
                 : "a"(s.K[0]), "a"(s.K[1]), "a"(s.K[4]), "a"(s.K[5]), "a"(s.K[8]), "a"(s.K[9]), "a"(s.K[12]), "a"(s.K[13]),
                   "v"(bq[0]), "v"(bq[1]), "v"(bq[2]), "v"(bq[3]));
  } else if constexpr (P == 8) {
    asm volatile("s_nop 5\n\t" FOLD("%0", "%1", "%2", "%6", ROT3, "8") "s_nop 0\n\t" FOLD("%0", "%1", "%3", "%7", ROT2, "8") "s_nop 0\n\t"
                 FOLD("%0", "%1", "%4", "%8", ROT1, "8") "s_nop 0\n\t" FOLD("%0", "%1", "%5", "%9", ROT0, "8")
                 : "+v"(res[2]), "+v"(res[3])
                 : "v"(d[1][0][0]), "v"(d[1][0][1]), "v"(d[1][0][2]), "v"(d[1][0][3]), "v"(d[1][1][0]), "v"(d[1][1][1]), "v"(d[1][1][2]), "v"(d[1][1][3]));
  } else if constexpr (ps == 0) QKP(ROT0, ROT3, ROT2, ROT1, "1");
  else if constexpr (ps == 1) QKP(ROT1, ROT0, ROT3, ROT2, "2");
  else if constexpr (ps == 2) QKP(ROT2, ROT1, ROT0, ROT3, "4");
  else QKP(ROT3, ROT2, ROT1, ROT0, "8");
}
template <int N> DI u32 bcast(u32 x) { return (u32)__builtin_amdgcn_mov_dpp((int)x, 0x150 + N, 0xf, 0xf, true); }

struct Pv { v4u a[2]; u32 xb; f32x4 d1, d2; v4u t[2][4]; u32 X[2], sel[4]; };
template <int J> DI void pv_step(St& s, Pv& p) {
  if constexpr ((J & 1) == 0 && J < 12) { issue_v<10 + J / 2>(s); }
  constexpr int G = (11 + J / 2) < 16 ? (11 + J / 2) : 16;
  if constexpr (J + 2 < 9) VMWAIT(4 * (G - (J + 3)));
  if constexpr (J >= 9) {
    VMWAIT(4 * (G - (J + 1)));
    asm volatile("" : "+v"(s.V[(J - 9) * 4]), "+v"(s.V[(J - 9) * 4 + 1]), "+v"(s.V[(J - 9) * 4 + 2]), "+v"(s.V[(J - 9) * 4 + 3]));
  }
  GAP();
#define VT(i) (J < 9 ? p.t[J & 1][i] : s.V[J < 9 ? 0 : (J - 9) * 4 + (i)])
#define RD(i) do { if constexpr (J + 2 < 9) p.t[J & 1][i] = lds_ld<v4u>(s.lbase + s.voff + ((J + 2) * 4 + (i)) * 1024); } while (0)
  mf_v(p.d1, p.a[0], VT(0)); GAP();
  p.a[1][1] = __builtin_amdgcn_perm(p.xb, p.xb, p.sel[1]); p.a[1][2] = __builtin_amdgcn_perm(p.xb, p.xb, p.sel[2]); p.a[1][3] = __builtin_amdgcn_perm(p.xb, p.xb, p.sel[3]);
  RD(0); GAP();
  mf_v(p.d2, p.a[0], VT(1)); GAP();
  if constexpr (J < 15) { p.xb = bcast<(J + 1) & 15>(p.X[0]); p.a[0][0] = __builtin_amdgcn_perm(p.xb, p.xb, p.sel[0]); } else asm volatile("s_nop 0");
  RD(1); GAP();
  mf_v(p.d1, p.a[1], VT(2)); GAP();
  if constexpr (J < 15) { p.a[0][1] = __builtin_amdgcn_perm(p.xb, p.xb, p.sel[1]); p.a[0][2] = __builtin_amdgcn_perm(p.xb, p.xb, p.sel[2]); p.a[0][3] = __builtin_amdgcn_perm(p.xb, p.xb, p.sel[3]); }
  RD(2); GAP();
  mf_v(p.d2, p.a[1], VT(3)); GAP();
  if constexpr (J < 15) { p.xb = bcast<(J + 1) & 15>(p.X[1]); p.a[1][0] = __builtin_amdgcn_perm(p.xb, p.xb, p.sel[0]); }
  RD(3); GAP();
#undef VT
#undef RD
}

__global__ void __launch_bounds__(256, 1)
mega(const char* x, const char* kc, const char* vc, const char* wq, const char* wo, const char* w1, const char* b1,
     const char* w2, const char* b2, char* out, char* ws) {
  extern __shared__ __attribute__((aligned(16))) char smem[];
  STAMP(0);
  const u32 t_start = (u32)__builtin_amdgcn_s_memrealtime();
#define HOLD(k) do { if (WAIT_AT == (k)) { while ((u32)__builtin_amdgcn_s_memrealtime() - t_start < WAIT_US * 100u) __builtin_amdgcn_s_sleep(1); } } while (0)
  const int lane = threadIdx.x & 63, w = __builtin_amdgcn_readfirstlane(threadIdx.x >> 6), g = lane >> 4, n = lane & 15;
  const int wg = blockIdx.x, xg = wg & 7, r = wg >> 3, sub = r & 3;
  const int bh = (wg & ~7) | ((wg + r) & 7), b = bh >> 5, h = bh & 31;
  const int grp = xg * 4 + ((r - 16) >> 2);
  const bool isA = r < 16, isC = !isA && grp < 20, isE = !isA && grp >= 20 && grp < 30;
  const bool isD = isA && r < (xg < 3 ? 16 : 12);
  const int aidx = xg * 16 + r, fidx = (xg < 3 ? 16 * xg : 48 + 12 * (xg - 3)) + r;
  const int kh = grp >= 10 ? 1 : 0, cs = (isC ? grp - 10 * kh : grp - 20) * 4 + sub;
  const u32 lds0 = (u32)(size_t)(LAS char*)smem;
  St s;
  s.voff = lane * 16; s.lbase = lds0 + w * LDS_WAVE;
  const size_t kvoff = (size_t)bh * 1048576 + 786432 + w * 4096;
  const char* kb = kc + kvoff;
  s.vb = vc + kvoff;
  const u32 koff = (n >> 2) * 256 + g * 64 + (n & 3) * 16;
  const int eh = lane >> 5, eb = (lane >> 2) & 7, ec = lane & 3, ecol = 8 * eh + 2 * ec;

  u32 epoch, aux;
  {
    const u32 z = 0;
    asm volatile("global_load_dword %0, %1, %2 sc1" : "=v"(epoch) : "v"(z), "s"(ws) : "memory");
    const char* ap = isD ? b1 + fidx * 32 : isE ? b2 + cs * 32 : isC ? x + cs * 32 : x;
    const u32 ao = ecol * 2 + (isC ? eb * 1280 : 0);
    asm volatile("global_load_dword %0, %1, %2" : "=v"(aux) : "v"(ao), "s"(ap) : "memory");
  }
  if (w == 0) {
    const u32 rblk = isC ? WS_XP + (cs * 2 + kh) * 256 : isD ? WS_H + fidx * 256 : isE ? WS_X2 + cs * 256 : WS_DUMMY;
    const u32 rl = isC ? XP_L : isD ? H_L : 0;
    const u32 ro = g < 2 ? WS_O + g * O_L + h * 2048 + b * 128 + (n >> 3) * 1024 + (n & 7) * 16 : rblk + (g - 2) * rl + n * 16;
    const v4u sv = {SENT, SENT, SENT, SENT};
    asm volatile("global_store_dwordx4 %0, %1, %2 sc1\n\ts_nop 1" ::"v"(ro), "v"(sv), "s"(ws) : "memory");
  }
  if (w == 0 && isE) { const u32 zz = 0; const u32 oo = eb * 1280 + cs * 32 + ecol * 2; asm volatile("global_store_dword %0, %1, %2 sc1" ::"v"(oo), "v"(zz), "s"(out) : "memory"); }
  v4u B[5];
#pragma unroll
  for (int i = 0; i < 5; i++) B[i] = v4u{ONES, ONES, ONES, ONES};
  auto dmaW = [&](const char* base, u32 o, int cnt, int mod, int start, int step, u32 ldst) {
    int ii = start;
    for (int i = 0; i < cnt; i++) {
      const char* p = base + ii * step;
      const u32 m0v = ldst + ii * 1024;
      asm volatile("s_mov_b32 m0, %2\n\ts_nop 0\n\tglobal_load_lds_dwordx4 %0, %1 nt" ::"v"(o), "s"(p), "s"(m0v) : "memory");
      ii++; if (ii == mod) ii = 0;
    }
  };
  if (isA) {
    const u32 xo = (n & 7) * 1280 + g * 16;
    const char* xb = x + w * 320;
    u64 sv; const u64 mk = 0x00FF00FF00FF00FFull;
    asm volatile("s_mov_b64 %5, exec\n\ts_mov_b64 exec, %8\n\t"
                 "global_load_dwordx4 %0, %6, %7\n\tglobal_load_dwordx4 %1, %6, %7 offset:64\n\tglobal_load_dwordx4 %2, %6, %7 offset:128\n\t"
                 "global_load_dwordx4 %3, %6, %7 offset:192\n\tglobal_load_dwordx4 %4, %6, %7 offset:256\n\ts_mov_b64 exec, %5"
                 : "+v"(B[0]), "+v"(B[1]), "+v"(B[2]), "+v"(B[3]), "+v"(B[4]), "=&s"(sv) : "v"(xo), "s"(xb), "s"(mk) : "memory");
    dmaW(wq + (size_t)(w * 160) * 8192 + aidx * 64, (lane >> 2) * 8192 + (lane & 3) * 16, 10, 10, 5 * (r & 1), 16 * 8192, s.lbase);
    if (isD) dmaW(w1 + (size_t)(w * 160) * 3456 + fidx * 32, (lane >> 1) * 3456 + (lane & 1) * 16, 5, 5, sub, 32 * 3456, s.lbase + 10240);
  } else if (isC) {
    dmaW(wo + (size_t)(kh * 2048 + w * 512) * 1280 + cs * 32, (lane >> 1) * 1280 + (lane & 1) * 16, 16, 16, 4 * sub, 32 * 1280, s.lbase);
  } else if (isE) {
    const int cnt = w == 3 ? 12 : 14;
    dmaW(w2 + (size_t)(w * 448) * 1280 + cs * 32, (lane >> 1) * 1280 + (lane & 1) * 16, cnt, cnt, 3 * sub, 32 * 1280, s.lbase);
  }
#pragma unroll
  for (int j = 0; j < 4; j++) { const char* base = kb + j * 16384; LD4("=a", s.K, 4 * j, koff, base); }
  asm volatile("s_waitcnt vmcnt(16)" : "+v"(epoch), "+v"(aux), "+v"(B[0]), "+v"(B[1]), "+v"(B[2]), "+v"(B[3]), "+v"(B[4])::"memory");
  const u32 tag = __builtin_amdgcn_readfirstlane(epoch) + 1u;
  if (w == 0 && lane == 0) st_sc1(ws, WS_FLAGS + wg * 4, tag);
  v4u W[16];
  {
    const int q4 = (lane >> 2) & 3, p4 = lane & 3;
    if (isA) {
      const u32 a0 = s.lbase + (8 * g + q4) * 64 + 8 * p4, d0 = s.lbase + 10240 + (8 * g + q4) * 32 + 8 * p4;
#pragma unroll
      for (int i = 0; i < 10; i++) W[i] = tr_frag(a0 + (i >> 1) * 2048 + (i & 1) * 32, 256);
#pragma unroll
      for (int i = 0; i < 5; i++) W[10 + i] = tr_frag(d0 + i * 1024, 128);
      W[15] = v4u{ONES, ONES, ONES, ONES};
    } else {
      const u32 c0 = s.lbase + (32 * (p4 >> 1) + 8 * g + q4) * 32 + 8 * (p4 & 1);
#pragma unroll
      for (int i = 0; i < 16; i++) W[i] = tr_frag(c0 + (i >> 1) * 2048 + (i & 1) * 16, 128);
    }
  }
  asm volatile("s_waitcnt lgkmcnt(0)" ::: "memory");
  STAMP(1);
  f32x2 csD = {0.f, 0.f};
  float res_lo = bf_lo(aux), res_hi = bf_hi(aux);
  float xr_lo = 0.f, xr_hi = 0.f;

#pragma unroll 1
  for (int l = 0; l < 2; l++) {
    const bool first = l == 0;
    OPAQUE(ws);
    if (isA) {
      LANES();
      if (!first) {
#pragma unroll
        for (int i = 0; i < 5; i++) B[i] = v4u{ONES, ONES, ONES, ONES};
        const u32 xo = g * 128 + (n & 7) * 16;
        const char* xb = ws + WS_X2 + w * 2560;
        for (int it = 0; it < SPIN_CAP; it++) {
          u64 sv; const u64 mk = 0x00FF00FF00FF00FFull;
          asm volatile("s_mov_b64 %5, exec\n\ts_mov_b64 exec, %8\n\t"
                       "global_load_dwordx4 %0, %6, %7 sc1\n\tglobal_load_dwordx4 %1, %6, %7 offset:512 sc1\n\tglobal_load_dwordx4 %2, %6, %7 offset:1024 sc1\n\t"
                       "global_load_dwordx4 %3, %6, %7 offset:1536 sc1\n\tglobal_load_dwordx4 %4, %6, %7 offset:2048 sc1\n\ts_mov_b64 exec, %5\n\ts_waitcnt vmcnt(0)"
                       : "+v"(B[0]), "+v"(B[1]), "+v"(B[2]), "+v"(B[3]), "+v"(B[4]), "=&s"(sv) : "v"(xo), "s"(xb), "s"(mk) : "memory");
          const u32 mx = max(max(max(umax4(B[0]), umax4(B[1])), max(umax4(B[2]), umax4(B[3]))), umax4(B[4]));
          if (!__any(mx == SENT)) break;
        }
        STAMP(9);
      }
      f32x4 t0, t1, gr;
      asm volatile("s_nop 1\n\t" MF "%0, %3, %13, 0\n\t" MF "%1, %4, %13, 0\n\t" MF "%2, %13, %13, 0\n\t"
                   MF "%0, %5, %14, %0\n\t" MF "%1, %6, %14, %1\n\t" MF "%2, %14, %14, %2\n\t"
                   MF "%0, %7, %15, %0\n\t" MF "%1, %8, %15, %1\n\t" MF "%2, %15, %15, %2\n\t"
                   MF "%0, %9, %16, %0\n\t" MF "%1, %10, %16, %1\n\t" MF "%2, %16, %16, %2\n\t"
                   MF "%0, %11, %17, %0\n\t" MF "%1, %12, %17, %1\n\t" MF "%2, %17, %17, %2\n\ts_nop 7"
                   : "=&v"(t0), "=&v"(t1), "=&v"(gr)
                   : "v"(W[0]), "v"(W[1]), "v"(W[2]), "v"(W[3]), "v"(W[4]), "v"(W[5]), "v"(W[6]), "v"(W[7]), "v"(W[8]), "v"(W[9]),
                     "v"(B[0]), "v"(B[1]), "v"(B[2]), "v"(B[3]), "v"(B[4]));
      if (n <= 8) {
        const u32 a = n < 8 ? lds0 + SC_ROLE + w * 1024 + (g >> 1) * 256 + n * 32 + (g & 1) * 16 : lds0 + SC_CS + w * 128 + g * 16;
        lds_st<f32x4>(a, t0);
        lds_st<f32x4>(a + (n < 8 ? 512 : 64), t1);
      }
      if (n < 8 && (g == 2 || g == (n >> 2))) {
        const float gv = g == 2 ? gr[0] : (n & 3) == 0 ? gr[0] : (n & 3) == 1 ? gr[1] : (n & 3) == 2 ? gr[2] : gr[3];
        lds_st<float>(lds0 + SC_STAT + w * 64 + n * 8 + (g == 2 ? 4 : 0), gv);
      }
      if (first) {
        f32x4 c1;
        asm volatile("s_nop 1\n\t" MF "%0, %1, %6, 0\n\t" MF "%0, %2, %6, %0\n\t" MF "%0, %3, %6, %0\n\t" MF "%0, %4, %6, %0\n\t" MF "%0, %5, %6, %0\n\ts_nop 7"
                     : "=&v"(c1) : "v"(W[10]), "v"(W[11]), "v"(W[12]), "v"(W[13]), "v"(W[14]), "v"(W[15]));
        if (n == 0) lds_st<f32x4>(lds0 + SC_CS + 512 + w * 64 + g * 16, c1);
      }
      lds_barrier();
      if (w < 2) {
        f32x2 acc = {0.f, 0.f}, csum = {0.f, 0.f}, st = {0.f, 0.f};
#pragma unroll
        for (int ww = 0; ww < 4; ww++) {
          acc += lds_ld<f32x2>(lds0 + SC_ROLE + ww * 1024 + w * 512 + lane * 8);
          csum += lds_ld<f32x2>(lds0 + SC_CS + ww * 128 + w * 64 + ecol * 4);
          st += lds_ld<f32x2>(lds0 + SC_STAT + ww * 64 + eb * 8);
        }
        if (first) {
#pragma unroll
          for (int ww = 0; ww < 4; ww++) csD += lds_ld<f32x2>(lds0 + SC_CS + 512 + ww * 64 + ecol * 4);
        }
        const float mean = st[1] * (1.f / 640.f), var = fmaxf(st[0] * (1.f / 640.f) - mean * mean, 0.f);
        const float rs = __builtin_amdgcn_rsqf(var + 1e-5f);
        const v2u gq = {pk_bf16(rs * (acc[0] - mean * csum[0]), rs * (acc[1] - mean * csum[1])), tag};
        st2_sc1(ws + WS_Q + l * Q_L, (eb * 128 + aidx) * 128 + (8 * w + 4 * eh + ec) * 8, gq);
      }
      if (!first) STAMP(10);
    }
    LANES();
    s.voff = lane * 16; s.vrun = s.voff; s.skip = l;
    OPAQUE(s.vb); OPAQUE(s.lbase);
    const u32 qoff = (4 * g + (n >> 2)) * 32;
    const char* qb = ws + WS_Q + l * Q_L + (b * 128 + h * 4) * 128;
    v4u qg0, qg1;
    bool ok = false;
    if (first) {
      const u32 koff2 = (n >> 2) * 256 + g * 64 + (n & 3) * 16;
      OPAQUE(kb);
#pragma unroll
      for (int j = 4; j < 16; j++) { const char* base = kb + j * 16384; LD4("+a", s.K, 4 * j, koff2, base); }
      v4u fl;
      const char* fb = ws + WS_FLAGS;
      asm volatile("global_load_dwordx4 %0, %3, %4 sc1\n\tglobal_load_dwordx4 %1, %3, %4 offset:16 sc1\n\tglobal_load_dwordx4 %2, %5, %6 sc1"
                   : "=&v"(qg0), "=&v"(qg1), "=&v"(fl) : "v"(qoff), "s"(qb), "v"(s.voff), "s"(fb) : "memory");
      issue_v<0>(s); issue_v<1>(s);
      asm volatile("s_waitcnt vmcnt(8)" : "+v"(qg0), "+v"(qg1), "+v"(fl)::"memory");
#pragma unroll 1
      for (int it = 0; it < SPIN_CAP; it++) {
        if (__all(fl[0] == tag && fl[1] == tag && fl[2] == tag && fl[3] == tag)) break;
        asm volatile("global_load_dwordx4 %0, %1, %2 sc1\n\ts_waitcnt vmcnt(0)" : "=&v"(fl) : "v"(s.voff), "s"(fb) : "memory");
      }
      ok = __all(qg0[1] == tag && qg0[3] == tag && qg1[1] == tag && qg1[3] == tag);
    } else asm volatile("" : "=v"(qg0), "=v"(qg1));
#pragma unroll 1
    for (int it = 0; !ok && it < SPIN_CAP; it++) {
      asm volatile("global_load_dwordx4 %0, %2, %3 sc1\n\tglobal_load_dwordx4 %1, %2, %3 offset:16 sc1\n\ts_waitcnt vmcnt(0)"
                   : "=&v"(qg0), "=&v"(qg1) : "v"(qoff), "s"(qb) : "memory");
      ok = __all(qg0[1] == tag && qg0[3] == tag && qg1[1] == tag && qg1[3] == tag);
    }
    if (first) STAMP(2); else STAMP(11);
    float res[4] = {0.f, 0.f, 0.f, 0.f};
    {
      v4u bq[4];
#pragma unroll
      for (int u = 0; u < 4; u++) { const bool on = (n & 3) == u; bq[u] = v4u{on ? qg0[0] : 0u, on ? qg0[2] : 0u, on ? qg1[0] : 0u, on ? qg1[2] : 0u}; }
      asm volatile("s_nop 1" : "+v"(bq[0]), "+v"(bq[1]), "+v"(bq[2]), "+v"(bq[3]), "+v"(res[0]), "+v"(res[1]), "+v"(res[2]), "+v"(res[3]));
      f32x4 d[2][2];
      issue_v<2>(s);
      qk_period<0>(s, bq, d, res); issue_v<3>(s);
      qk_period<1>(s, bq, d, res); issue_v<4>(s);
      qk_period<2>(s, bq, d, res); issue_v<5>(s);
      qk_period<3>(s, bq, d, res); issue_v<6>(s);
      qk_period<4>(s, bq, d, res); issue_v<7>(s);
      qk_period<5>(s, bq, d, res); issue_v<8>(s);
      qk_period<6>(s, bq, d, res); issue_v<9>(s);
      qk_period<7>(s, bq, d, res);
      qk_period<8>(s, bq, d, res);
    }
    Pv p;
    VMWAIT(32);
#pragma unroll
    for (int i = 0; i < 8; i++) p.t[i >> 2][i & 3] = lds_ld<v4u>(s.lbase + s.voff + i * 1024);
    constexpr float C2 = 0.08838834764831845f * 1.4426950408889634f;
    const float mw = wave_max(fmaxf(fmaxf(res[0], res[1]), fmaxf(res[2], res[3]))) * C2;
    float pr[4];
#pragma unroll
    for (int z = 0; z < 4; z++) pr[z] = __builtin_amdgcn_exp2f(__builtin_fmaf(res[z], C2, -mw));
    p.X[0] = pk_bf16(pr[0], pr[1]); p.X[1] = pk_bf16(pr[2], pr[3]);
    const float lw = wave_sum((pr[0] + pr[1]) + (pr[2] + pr[3]));
#pragma unroll
    for (int rr = 0; rr < 4; rr++) {
      const u32 src = n < 8 ? 0x0100u : 0x0302u;
      p.sel[rr] = ((n & 7) >> 1) != rr ? 0x0c0c0c0cu : (n & 1) ? (src << 16) | 0x0c0cu : 0x0c0c0000u | src;
    }
    p.d1 = f32x4{0.f, 0.f, 0.f, 0.f}; p.d2 = p.d1;
    p.xb = bcast<0>(p.X[0]);
#pragma unroll
    for (int rr = 0; rr < 4; rr++) p.a[0][rr] = __builtin_amdgcn_perm(p.xb, p.xb, p.sel[rr]);
    p.xb = bcast<0>(p.X[1]);
    p.a[1][0] = __builtin_amdgcn_perm(p.xb, p.xb, p.sel[0]);
    asm volatile("s_nop 1" : "+v"(p.a[0]), "+v"(p.d1), "+v"(p.d2));
    GAP();
    pv_step<0>(s, p); pv_step<1>(s, p); pv_step<2>(s, p); pv_step<3>(s, p);
    pv_step<4>(s, p); pv_step<5>(s, p); pv_step<6>(s, p); pv_step<7>(s, p);
    pv_step<8>(s, p); pv_step<9>(s, p); pv_step<10>(s, p); pv_step<11>(s, p);
    pv_step<12>(s, p); pv_step<13>(s, p); pv_step<14>(s, p); pv_step<15>(s, p);
    asm volatile("s_nop 7" : "+v"(p.d1), "+v"(p.d2));
    if (first) STAMP(3); else STAMP(12);
    {
    LANES();
    lds_st<f32x4>(lds0 + SC_ATT + (w * 2 + (g >> 1)) * 512 + n * 32 + (g & 1) * 16, g < 2 ? p.d1 : p.d2);
    if (lane == 0) { lds_st<float>(lds0 + SC_ML + w * 4, mw); lds_st<float>(lds0 + SC_ML + 16 + w * 4, lw); }
    lds_barrier();
    if (w == 0) {
      const f32x4 m4 = lds_ld<f32x4>(lds0 + SC_ML), l4 = lds_ld<f32x4>(lds0 + SC_ML + 16);
      const float M = fmaxf(fmaxf(m4[0], m4[1]), fmaxf(m4[2], m4[3]));
      f32x2 o = {0.f, 0.f}; float L = 0.f;
#pragma unroll
      for (int ww = 0; ww < 4; ww++) {
        const float f = __builtin_amdgcn_exp2f(m4[ww] - M);
        L += f * l4[ww];
        o += f * (lds_ld<f32x2>(lds0 + SC_ATT + ww * 1024 + lane * 8) + lds_ld<f32x2>(lds0 + SC_ATT + ww * 1024 + 512 + lane * 8));
      }
      const float inv = __builtin_amdgcn_rcpf(L);
      st_sc1(ws + WS_O + l * O_L + h * 2048 + b * 128, eh * 1024 + (lane & 31) * 4, min(pk_bf16(o[0] * inv, o[1] * inv), SENT - 1));
    }
    }
    if (first) STAMP(4); else STAMP(13);
    if (isC || isE || isD) {
      LANES();
      OPAQUE(ws);
      if (isD) {
        v4u B[5];
        f32x4 t0, t1, t2;
        const char* xb = ws + WS_XP + l * XP_L + w * 5120 + 2048;
        const u32 xo = (g >> 1) * 512 + (n >> 3) * 256 + (g & 1) * 128 + (n & 7) * 16;
        for (int it = 0; it < SPIN_CAP; it++) {
          asm volatile("global_load_dwordx4 %0, %5, %6 offset:-2048 sc1\n\tglobal_load_dwordx4 %1, %5, %6 offset:-1024 sc1\n\t"
                       "global_load_dwordx4 %2, %5, %6 sc1\n\tglobal_load_dwordx4 %3, %5, %6 offset:1024 sc1\n\t"
                       "global_load_dwordx4 %4, %5, %6 offset:2048 sc1\n\ts_waitcnt vmcnt(0)"
                       : "=&v"(B[0]), "=&v"(B[1]), "=&v"(B[2]), "=&v"(B[3]), "=&v"(B[4]) : "v"(xo), "s"(xb) : "memory");
          const u32 mx = max(max(max(umax4(B[0]), umax4(B[1])), max(umax4(B[2]), umax4(B[3]))), umax4(B[4]));
          if (!__any(mx == SENT)) break;
        }
        if (first) STAMP(5); else STAMP(14);
        asm volatile("s_nop 1\n\t" MF "%0, %3, %9, 0\n\t" MF "%1, %9, %9, 0\n\t" MF "%2, %8, %9, 0\n\t"
                     MF "%0, %4, %10, %0\n\t" MF "%1, %10, %10, %1\n\t" MF "%2, %8, %10, %2\n\t"
                     MF "%0, %5, %11, %0\n\t" MF "%1, %11, %11, %1\n\t" MF "%2, %8, %11, %2\n\t"
                     MF "%0, %6, %12, %0\n\t" MF "%1, %12, %12, %1\n\t" MF "%2, %8, %12, %2\n\t"
                     MF "%0, %7, %13, %0\n\t" MF "%1, %13, %13, %1\n\t" MF "%2, %8, %13, %2\n\ts_nop 7"
                     : "=&v"(t0), "=&v"(t1), "=&v"(t2)
                     : "v"(W[10]), "v"(W[11]), "v"(W[12]), "v"(W[13]), "v"(W[14]), "v"(W[15]), "v"(B[0]), "v"(B[1]), "v"(B[2]), "v"(B[3]), "v"(B[4]));
        lds_st<f32x4>(lds0 + SC_ROLE + (w * 2 + (n >> 3)) * 512 + (g >> 1) * 256 + (n & 7) * 32 + (g & 1) * 16, t0);
        const bool isG = (g & 1) == ((n >> 2) & 1);
        const float gv = !isG ? t2[0] : (n & 3) == 0 ? t1[0] : (n & 3) == 1 ? t1[1] : (n & 3) == 2 ? t1[2] : t1[3];
        lds_st<float>(lds0 + SC_STAT + w * 256 + (n & 7) * 32 + (isG ? 0 : 16) + (g >> 1) * 8 + (n >> 3) * 4, gv);
      } else {
        if (isE && w == 0) {
          const char* xb = ws + WS_XP + l * XP_L + cs * 512;
          const u32 xo = lane * 4;
          u32 p1, p2;
          for (int it = 0; it < SPIN_CAP; it++) {
            asm volatile("global_load_dword %0, %2, %3 sc1\n\tglobal_load_dword %1, %2, %3 offset:256 sc1\n\ts_waitcnt vmcnt(0)"
                         : "=&v"(p1), "=&v"(p2) : "v"(xo), "s"(xb) : "memory");
            if (!__any(max(p1, p2) == SENT)) break;
          }
          xr_lo = bf_lo(p1) + bf_lo(p2); xr_hi = bf_hi(p1) + bf_hi(p2);
        }
        v4u B[8];
        f32x4 t0, t1;
        B[6] = v4u{0u, 0u, 0u, 0u}; B[7] = B[6];
        const int extra = __builtin_amdgcn_readfirstlane(isC ? 2 : (w == 3 ? 0 : 1));
        const char* gb = ws + (isC ? WS_O + l * O_L + (kh * 16 + 4 * w) * 2048 : WS_H + l * H_L + w * 7168) + 4096;
        const u32 go = isC ? (n & 7) * 128 + (n >> 3) * 64 + g * 16 : (n >> 3) * 512 + g * 128 + (n & 7) * 16;
        for (int it = 0; it < SPIN_CAP; it++) {
          asm volatile("global_load_dwordx4 %0, %8, %9 offset:-4096 sc1\n\tglobal_load_dwordx4 %1, %8, %9 offset:-3072 sc1\n\t"
                       "global_load_dwordx4 %2, %8, %9 offset:-2048 sc1\n\tglobal_load_dwordx4 %3, %8, %9 offset:-1024 sc1\n\t"
                       "global_load_dwordx4 %4, %8, %9 sc1\n\tglobal_load_dwordx4 %5, %8, %9 offset:1024 sc1\n\t"
                       "s_cmp_lt_i32 %10, 1\n\ts_cbranch_scc1 .Ly%=\n\tglobal_load_dwordx4 %6, %8, %9 offset:2048 sc1\n\t"
                       "s_cmp_lt_i32 %10, 2\n\ts_cbranch_scc1 .Ly%=\n\tglobal_load_dwordx4 %7, %8, %9 offset:3072 sc1\n.Ly%=:\n\ts_waitcnt vmcnt(0)"
                       : "=&v"(B[0]), "=&v"(B[1]), "=&v"(B[2]), "=&v"(B[3]), "=&v"(B[4]), "=&v"(B[5]), "+v"(B[6]), "+v"(B[7])
                       : "v"(go), "s"(gb), "s"(extra) : "memory", "scc");
          const u32 mx = max(max(max(umax4(B[0]), umax4(B[1])), max(umax4(B[2]), umax4(B[3]))), max(max(umax4(B[4]), umax4(B[5])), max(umax4(B[6]), umax4(B[7]))));
          if (!__any(mx == SENT)) break;
        }
        if (first) STAMP(5); else STAMP(14);
        asm volatile("s_nop 1\n\t" MF "%0, %2, %18, 0\n\t" MF "%1, %3, %18, 0\n\t" MF "%0, %4, %19, %0\n\t" MF "%1, %5, %19, %1\n\t"
                     MF "%0, %6, %20, %0\n\t" MF "%1, %7, %20, %1\n\t" MF "%0, %8, %21, %0\n\t" MF "%1, %9, %21, %1\n\t"
                     MF "%0, %10, %22, %0\n\t" MF "%1, %11, %22, %1\n\t" MF "%0, %12, %23, %0\n\t" MF "%1, %13, %23, %1\n\t"
                     "s_cmp_lt_i32 %26, 1\n\ts_cbranch_scc1 .Lx%=\n\t" MF "%0, %14, %24, %0\n\t" MF "%1, %15, %24, %1\n\t"
                     "s_cmp_lt_i32 %26, 2\n\ts_cbranch_scc1 .Lx%=\n\t" MF "%0, %16, %25, %0\n\t" MF "%1, %17, %25, %1\n.Lx%=:\n\ts_nop 7"
                     : "=&v"(t0), "=&v"(t1)
                     : "v"(W[0]), "v"(W[1]), "v"(W[2]), "v"(W[3]), "v"(W[4]), "v"(W[5]), "v"(W[6]), "v"(W[7]), "v"(W[8]), "v"(W[9]), "v"(W[10]), "v"(W[11]),
                       "v"(W[12]), "v"(W[13]), "v"(W[14]), "v"(W[15]),
                       "v"(B[0]), "v"(B[1]), "v"(B[2]), "v"(B[3]), "v"(B[4]), "v"(B[5]), "v"(B[6]), "v"(B[7]), "s"(extra) : "scc");
        if ((g >> 1) == (n >> 3)) {
          const u32 a = lds0 + SC_ROLE + (w * 2 + (n >> 3)) * 512 + (n & 7) * 32 + (g & 1) * 16;
          lds_st<f32x4>(a, t0);
          lds_st<f32x4>(a + 256, t1);
        }
      }
      lds_barrier();
      if (w == 0) {
        f32x2 acc = {0.f, 0.f};
#pragma unroll
        for (int i = 0; i < 8; i++) acc += lds_ld<f32x2>(lds0 + SC_ROLE + i * 512 + lane * 8);
        if (isD) {
          float ss = 0.f, sm = 0.f;
#pragma unroll
          for (int ww = 0; ww < 4; ww++) {
            const f32x4 gq = lds_ld<f32x4>(lds0 + SC_STAT + ww * 256 + eb * 32);
            const f32x2 rq = lds_ld<f32x2>(lds0 + SC_STAT + ww * 256 + eb * 32 + 16);
            ss += (gq[0] + gq[1]) + (gq[2] + gq[3]); sm += rq[0] + rq[1];
          }
          const float mean = sm * (1.f / 640.f), var = fmaxf(ss * (1.f / 640.f) - mean * mean, 0.f);
          const float r1 = __builtin_amdgcn_rsqf(var + 1e-5f);
          const float rs = r1 * __builtin_amdgcn_rsqf(var * r1 * r1 + 1e-5f);
          const float y0 = rs * (acc[0] - mean * csD[0]) + res_lo, y1 = rs * (acc[1] - mean * csD[1]) + res_hi;
          constexpr float GC = -2.f * 0.7978845608028654f * 1.4426950408889634f;
          const float e0 = __builtin_amdgcn_exp2f(GC * (y0 + 0.044715f * y0 * y0 * y0)), e1 = __builtin_amdgcn_exp2f(GC * (y1 + 0.044715f * y1 * y1 * y1));
          const u32 hv = pk_bf16(y0 * __builtin_amdgcn_rcpf(1.f + e0), y1 * __builtin_amdgcn_rcpf(1.f + e1));
          st_sc1(ws + WS_H + l * H_L + fidx * 256, lane * 4, min(hv, SENT - 1));
        } else if (isC) {
          const u32 xv = kh ? pk_bf16(acc[0], acc[1]) : pk_bf16(acc[0] + res_lo, acc[1] + res_hi);
          st_sc1(ws + WS_XP + l * XP_L + (cs * 2 + kh) * 256, lane * 4, min(xv, SENT - 1));
          if (first && kh == 0) {
            const char* xb = ws + WS_X2 + cs * 256;
            const u32 xo = lane * 4;
            u32 p1;
            for (int it = 0; it < SPIN_CAP; it++) {
              asm volatile("global_load_dword %0, %1, %2 sc1\n\ts_waitcnt vmcnt(0)" : "=&v"(p1) : "v"(xo), "s"(xb) : "memory");
              if (!__any(p1 == SENT)) break;
            }
            res_lo = bf_lo(p1); res_hi = bf_hi(p1);
          }
        } else {
          const u32 xv = pk_bf16(xr_lo + acc[0] + res_lo, xr_hi + acc[1] + res_hi);
          if (first) st_sc1(ws + WS_X2 + cs * 256, lane * 4, min(xv, SENT - 1));
          else {
            HOLD(2);
            st_sc1(out, eb * 1280 + cs * 32 + ecol * 2, xv);
            if (cs == 0 && lane == 0) st_sc1(ws, WS_EPOCH, tag);
            HOLD(4);
          }
        }
      }
      if (first) STAMP(6); else STAMP(15);
    }
    asm volatile("" : "=v"(B[0]), "=v"(B[1]), "=v"(B[2]), "=v"(B[3]), "=v"(B[4]));
  }
}

extern "C" void kernel_launch(void* const* d_in, const int* in_sizes, int n_in, void* d_out, int out_size, void* d_ws,
                              size_t ws_size, hipStream_t stream) {
  (void)hipFuncSetAttribute((const void*)mega, hipFuncAttributeMaxDynamicSharedMemorySize, LDS_BYTES);
  mega<<<256, 256, LDS_BYTES, stream>>>((const char*)d_in[0], (const char*)d_in[1], (const char*)d_in[2], (const char*)d_in[3],
                                        (const char*)d_in[4], (const char*)d_in[6], (const char*)d_in[7], (const char*)d_in[8],
                                        (const char*)d_in[9], (char*)d_out, (char*)d_ws);
}
```

```cpp
#include <hip/hip_runtime.h>
#include <stdint.h>

typedef unsigned u32;
typedef unsigned long long u64;
typedef u32 v4u __attribute__((ext_vector_type(4)));
typedef u32 v2u __attribute__((ext_vector_type(2)));
typedef float f32x4 __attribute__((ext_vector_type(4)));
typedef float f32x2 __attribute__((ext_vector_type(2)));
typedef short v4s __attribute__((ext_vector_type(4)));
typedef __bf16 bf16x2 __attribute__((ext_vector_type(2)));
#define LAS __attribute__((address_space(3)))
#define GAS __attribute__((address_space(1)))
#define DI __device__ __forceinline__
#define GAP() __builtin_amdgcn_sched_barrier(0)

constexpr u32 WS_EPOCH = 0, WS_FLAGS = 0x400, WS_DUMMY = 0x800;
constexpr u32 WS_Q = 0x1000, Q_L = 131072;
constexpr u32 WS_O = WS_Q + 2 * Q_L, O_L = 65536;
constexpr u32 WS_XP = WS_O + 2 * O_L, XP_L = 20480;
constexpr u32 WS_H = WS_XP + 2 * XP_L, H_L = 27648;
constexpr u32 WS_X2 = WS_H + 2 * H_L;
constexpr u32 WS_STAMP = 1u << 20;
constexpr u32 LDS_WAVE = 36864, LDS_SC = 4 * LDS_WAVE, SC_ATT = LDS_SC, SC_ML = SC_ATT + 4096, SC_ROLE = SC_ML + 256,
              SC_STAT = SC_ROLE + 4096, SC_CS = SC_STAT + 1024, LDS_BYTES = 163840;
constexpr u32 SENT = 0xFFFFFFFFu, ONES = 0x3F803F80u;
constexpr int SPIN_CAP = 1 << 17;

DI u32 pk_bf16(float lo, float hi) {
  f32x2 v = {lo, hi};
  return __builtin_bit_cast(u32, __builtin_convertvector(v, bf16x2));
}
DI float bf_lo(u32 x) { return __builtin_bit_cast(float, x << 16); }
DI float bf_hi(u32 x) { return __builtin_bit_cast(float, x & 0xffff0000u); }
DI u32 umax4(v4u a) { return max(max(a[0], a[1]), max(a[2], a[3])); }
template <int CTRL> DI float dppf(float x) {
  return __builtin_bit_cast(float, __builtin_amdgcn_mov_dpp(__builtin_bit_cast(int, x), CTRL, 0xf, 0xf, true));
}
DI float wave_max(float x) {
  x = fmaxf(x, dppf<0xB1>(x)); x = fmaxf(x, dppf<0x4E>(x)); x = fmaxf(x, dppf<0x124>(x)); x = fmaxf(x, dppf<0x128>(x));
  auto s = __builtin_amdgcn_permlane16_swap(__float_as_uint(x), __float_as_uint(x), false, false);
  x = fmaxf(__uint_as_float(s[0]), __uint_as_float(s[1]));
  auto t = __builtin_amdgcn_permlane32_swap(__float_as_uint(x), __float_as_uint(x), false, false);
  return fmaxf(__uint_as_float(t[0]), __uint_as_float(t[1]));
}
DI float wave_sum(float x) {
  x += dppf<0xB1>(x); x += dppf<0x4E>(x); x += dppf<0x124>(x); x += dppf<0x128>(x);
  auto s = __builtin_amdgcn_permlane16_swap(__float_as_uint(x), __float_as_uint(x), false, false);
  x = __uint_as_float(s[0]) + __uint_as_float(s[1]);
  auto t = __builtin_amdgcn_permlane32_swap(__float_as_uint(x), __float_as_uint(x), false, false);
  return __uint_as_float(t[0]) + __uint_as_float(t[1]);
}
DI void lds_barrier() { asm volatile("s_waitcnt lgkmcnt(0)\n\ts_barrier" ::: "memory"); }
DI void st_sc1(char* base, u32 off, u32 v) { asm volatile("global_store_dword %0, %1, %2 sc1" ::"v"(off), "v"(v), "s"(base) : "memory"); }
DI void st2_sc1(char* base, u32 off, v2u v) { asm volatile("global_store_dwordx2 %0, %1, %2 sc1" ::"v"(off), "v"(v), "s"(base) : "memory"); }
template <typename T> DI T lds_ld(u32 a) { return *(const LAS T*)(size_t)a; }
template <typename T> DI void lds_st(u32 a, T v) { *(LAS T*)(size_t)a = v; }
DI v4u tr_frag(u32 a, u32 second) {
  const v4s lo = __builtin_amdgcn_ds_read_tr16_b64_v4i16((LAS v4s*)(size_t)a);
  const v4s hi = __builtin_amdgcn_ds_read_tr16_b64_v4i16((LAS v4s*)(size_t)(a + second));
  const v2u l = __builtin_bit_cast(v2u, lo), h = __builtin_bit_cast(v2u, hi);
  return v4u{l[0], l[1], h[0], h[1]};
}
DI int lane_id() { int l; asm volatile("v_mbcnt_lo_u32_b32 %0, -1, 0\n\tv_mbcnt_hi_u32_b32 %0, -1, %0" : "=v"(l)); return l; }
#define LANES() const int lane = lane_id(), g = lane >> 4, n = lane & 15, eh = lane >> 5, eb = (lane >> 2) & 7, ec = lane & 3, ecol = 8 * eh + 2 * ec; (void)g; (void)n; (void)eb; (void)ecol
#define OPAQUE(p) asm volatile("" : "+s"(p))
#define MF "v_mfma_f32_16x16x32_bf16 "
#define VMWAIT(n) asm volatile("s_waitcnt vmcnt(%0)" ::"i"(n) : "memory")

#define LD4(C, A, i0, vo, base)                                                                                     \
  asm volatile("global_load_dwordx4 %0, %4, %5 nt\n\tglobal_load_dwordx4 %1, %4, %5 offset:1024 nt\n\t"               \
               "global_load_dwordx4 %2, %4, %5 offset:2048 nt\n\tglobal_load_dwordx4 %3, %4, %5 offset:3072 nt"       \
               : C(A[i0]), C(A[i0 + 1]), C(A[i0 + 2]), C(A[i0 + 3])                                                   \
               : "v"(vo), "s"(base)                                                                                   \
               : "memory")
#define DMA4(vo, base, m0v)                                                                                          \
  asm volatile("s_mov_b32 m0, %2\n\ts_nop 0\n\tglobal_load_lds_dwordx4 %0, %1 nt\n\t"                                  \
               "global_load_lds_dwordx4 %0, %1 offset:1024 nt\n\tglobal_load_lds_dwordx4 %0, %1 offset:2048 nt\n\t"    \
               "global_load_lds_dwordx4 %0, %1 offset:3072 nt" ::"v"(vo), "s"(base), "s"(m0v)                          \
               : "memory")

#define STAMP(i) do { } while (0)

struct St {
  v4u K[64], V[28];
  const char* vb;
  u32 voff, vrun, lbase, skip;
};
template <int J> DI void issue_v(St& s) {
  if constexpr (J < 9)
    asm volatile("s_cmp_lg_u32 %3, 0\n\ts_cbranch_scc1 .Lv%=\n\ts_add_u32 m0, %2, %4\n\ts_nop 0\n\tglobal_load_lds_dwordx4 %1, %0 nt\n\t"
                 "global_load_lds_dwordx4 %1, %0 offset:1024 nt\n\tglobal_load_lds_dwordx4 %1, %0 offset:2048 nt\n\t"
                 "global_load_lds_dwordx4 %1, %0 offset:3072 nt\n\tv_add_u32 %1, 0x4000, %1\n.Lv%=:"
                 : "+s"(s.vb), "+v"(s.vrun) : "s"(s.lbase), "s"(s.skip), "i"(J * 4096) : "memory", "scc");
  else
    asm volatile("s_cmp_lg_u32 %6, 0\n\ts_cbranch_scc1 .Lv%=\n\tglobal_load_dwordx4 %0, %5, %4 nt\n\tglobal_load_dwordx4 %1, %5, %4 offset:1024 nt\n\t"
                 "global_load_dwordx4 %2, %5, %4 offset:2048 nt\n\tglobal_load_dwordx4 %3, %5, %4 offset:3072 nt\n\tv_add_u32 %5, 0x4000, %5\n.Lv%=:"
                 : "+v"(s.V[(J - 9) * 4]), "+v"(s.V[(J - 9) * 4 + 1]), "+v"(s.V[(J - 9) * 4 + 2]), "+v"(s.V[(J - 9) * 4 + 3]), "+s"(s.vb), "+v"(s.vrun)
                 : "s"(s.skip) : "memory", "scc");
}
DI void mf_v(f32x4& d, const v4u& a, const v4u& b) { asm volatile(MF "%0, %1, %2, %0" : "+v"(d) : "v"(a), "v"(b)); }

#define ROT0 "quad_perm:[0,1,2,3]"
#define ROT1 "row_ror:4"
#define ROT2 "row_ror:8"
#define ROT3 "row_ror:12"
#define FOLD(ra, rb, da, db, rot, bank) "v_add_f32_dpp " ra ", " da ", " ra " " rot " row_mask:0xf bank_mask:" bank "\n\tv_add_f32_dpp " rb ", " db ", " rb " " rot " row_mask:0xf bank_mask:" bank "\n\t"
#define QKP(r0, r1, r2, r3, bank)                                                                                                     \
  asm volatile(MF "%0, %4, %12, 0\n\t" MF "%1, %5, %12, 0\n\t" MF "%0, %6, %13, %0\n\t" MF "%1, %7, %13, %1\n\t" MF "%0, %8, %14, %0\n\t" \
               FOLD("%2", "%3", "%16", "%20", r0, bank) MF "%1, %9, %14, %1\n\t" FOLD("%2", "%3", "%17", "%21", r1, bank)              \
               MF "%0, %10, %15, %0\n\t" FOLD("%2", "%3", "%18", "%22", r2, bank) MF "%1, %11, %15, %1\n\t" FOLD("%2", "%3", "%19", "%23", r3, bank) \
               : "=&v"(d[c][0]), "=&v"(d[c][1]), "+v"(res[pz]), "+v"(res[pz + 1])                                                       \
               : "a"(s.K[16 * sp + z]), "a"(s.K[16 * sp + z + 1]), "a"(s.K[16 * sp + 4 + z]), "a"(s.K[16 * sp + 5 + z]),                \
                 "a"(s.K[16 * sp + 8 + z]), "a"(s.K[16 * sp + 9 + z]), "a"(s.K[16 * sp + 12 + z]), "a"(s.K[16 * sp + 13 + z]),          \
                 "v"(bq[0]), "v"(bq[1]), "v"(bq[2]), "v"(bq[3]),                                                                        \
                 "v"(d[c ^ 1][0][0]), "v"(d[c ^ 1][0][1]), "v"(d[c ^ 1][0][2]), "v"(d[c ^ 1][0][3]),                                    \
                 "v"(d[c ^ 1][1][0]), "v"(d[c ^ 1][1][1]), "v"(d[c ^ 1][1][2]), "v"(d[c ^ 1][1][3]))
template <int P> DI void qk_period(St& s, const v4u (&bq)[4], f32x4 (&d)[2][2], float (&res)[4]) {
  constexpr int z = 2 * (P >> 2), sp = P & 3, c = P & 1, pz = P > 0 ? 2 * ((P - 1) >> 2) : 0, ps = (P - 1) & 3;
  if constexpr (P == 0) {
    asm volatile(MF "%0, %2, %10, 0\n\t" MF "%1, %3, %10, 0\n\t" MF "%0, %4, %11, %0\n\t" MF "%1, %5, %11, %1\n\t"
                 MF "%0, %6, %12, %0\n\t" MF "%1, %7, %12, %1\n\t" MF "%0, %8, %13, %0\n\t" MF "%1, %9, %13, %1"
                 : "=&v"(d[0][0]), "=&v"(d[0][1])
                 : "a"(s.K[0]), "a"(s.K[1]), "a"(s.K[4]), "a"(s.K[5]), "a"(s.K[8]), "a"(s.K[9]), "a"(s.K[12]), "a"(s.K[13]),
                   "v"(bq[0]), "v"(bq[1]), "v"(bq[2]), "v"(bq[3]));
  } else if constexpr (P == 8) {
    asm volatile("s_nop 5\n\t" FOLD("%0", "%1", "%2", "%6", ROT3, "8") "s_nop 0\n\t" FOLD("%0", "%1", "%3", "%7", ROT2, "8") "s_nop 0\n\t"
                 FOLD("%0", "%1", "%4", "%8", ROT1, "8") "s_nop 0\n\t" FOLD("%0", "%1", "%5", "%9", ROT0, "8")
                 : "+v"(res[2]), "+v"(res[3])
                 : "v"(d[1][0][0]), "v"(d[1][0][1]), "v"(d[1][0][2]), "v"(d[1][0][3]), "v"(d[1][1][0]), "v"(d[1][1][1]), "v"(d[1][1][2]), "v"(d[1][1][3]));
  } else if constexpr (ps == 0) QKP(ROT0, ROT3, ROT2, ROT1, "1");
  else if constexpr (ps == 1) QKP(ROT1, ROT0, ROT3, ROT2, "2");
  else if constexpr (ps == 2) QKP(ROT2, ROT1, ROT0, ROT3, "4");
  else QKP(ROT3, ROT2, ROT1, ROT0, "8");
}
template <int N> DI u32 bcast(u32 x) { return (u32)__builtin_amdgcn_mov_dpp((int)x, 0x150 + N, 0xf, 0xf, true); }

struct Pv { v4u a[2]; u32 xb; f32x4 d1, d2; v4u t[2][4]; u32 X[2], sel[4]; };
template <int J> DI void pv_step(St& s, Pv& p) {
  if constexpr ((J & 1) == 0 && J < 12) { issue_v<10 + J / 2>(s); }
  constexpr int G = (11 + J / 2) < 16 ? (11 + J / 2) : 16;
  if constexpr (J + 2 < 9) VMWAIT(4 * (G - (J + 3)));
  if constexpr (J >= 9) {
    VMWAIT(4 * (G - (J + 1)));
    asm volatile("" : "+v"(s.V[(J - 9) * 4]), "+v"(s.V[(J - 9) * 4 + 1]), "+v"(s.V[(J - 9) * 4 + 2]), "+v"(s.V[(J - 9) * 4 + 3]));
  }
  GAP();
#define VT(i) (J < 9 ? p.t[J & 1][i] : s.V[J < 9 ? 0 : (J - 9) * 4 + (i)])
#define RD(i) do { if constexpr (J + 2 < 9) p.t[J & 1][i] = lds_ld<v4u>(s.lbase + s.voff + ((J + 2) * 4 + (i)) * 1024); } while (0)
  mf_v(p.d1, p.a[0], VT(0)); GAP();
  p.a[1][1] = __builtin_amdgcn_perm(p.xb, p.xb, p.sel[1]); p.a[1][2] = __builtin_amdgcn_perm(p.xb, p.xb, p.sel[2]); p.a[1][3] = __builtin_amdgcn_perm(p.xb, p.xb, p.sel[3]);
  RD(0); GAP();
  mf_v(p.d2, p.a[0], VT(1)); GAP();
  if constexpr (J < 15) { p.xb = bcast<(J + 1) & 15>(p.X[0]); p.a[0][0] = __builtin_amdgcn_perm(p.xb, p.xb, p.sel[0]); } else asm volatile("s_nop 0");
  RD(1); GAP();
  mf_v(p.d1, p.a[1], VT(2)); GAP();
  if constexpr (J < 15) { p.a[0][1] = __builtin_amdgcn_perm(p.xb, p.xb, p.sel[1]); p.a[0][2] = __builtin_amdgcn_perm(p.xb, p.xb, p.sel[2]); p.a[0][3] = __builtin_amdgcn_perm(p.xb, p.xb, p.sel[3]); }
  RD(2); GAP();
  mf_v(p.d2, p.a[1], VT(3)); GAP();
  if constexpr (J < 15) { p.xb = bcast<(J + 1) & 15>(p.X[1]); p.a[1][0] = __builtin_amdgcn_perm(p.xb, p.xb, p.sel[0]); }
  RD(3); GAP();
#undef VT
#undef RD
}

__global__ void __launch_bounds__(256, 1)
mega(const char* x, const char* kc, const char* vc, const char* wq, const char* wo, const char* w1, const char* b1,
     const char* w2, const char* b2, char* out, char* ws) {
  extern __shared__ __attribute__((aligned(16))) char smem[];
  STAMP(0);
  const int lane = threadIdx.x & 63, w = __builtin_amdgcn_readfirstlane(threadIdx.x >> 6), g = lane >> 4, n = lane & 15;
  const int wg = blockIdx.x, xg = wg & 7, r = wg >> 3, sub = r & 3;
  const int bh = (wg & ~7) | ((wg + r) & 7), b = bh >> 5, h = bh & 31;
  const int grp = xg * 4 + ((r - 16) >> 2);
  const bool isA = r < 16, isC = !isA && grp < 20, isE = !isA && grp >= 20 && grp < 30;
  const bool isD = isA && r < (xg < 3 ? 16 : 12);
  const int aidx = xg * 16 + r, fidx = (xg < 3 ? 16 * xg : 48 + 12 * (xg - 3)) + r;
  const int kh = grp >= 10 ? 1 : 0, cs = (isC ? grp - 10 * kh : grp - 20) * 4 + sub;
  const u32 lds0 = (u32)(size_t)(LAS char*)smem;
  St s;
  s.voff = lane * 16; s.lbase = lds0 + w * LDS_WAVE;
  const size_t kvoff = (size_t)bh * 1048576 + 786432 + w * 4096;
  const char* kb = kc + kvoff;
  s.vb = vc + kvoff;
  const u32 koff = (n >> 2) * 256 + g * 64 + (n & 3) * 16;
  const int eh = lane >> 5, eb = (lane >> 2) & 7, ec = lane & 3, ecol = 8 * eh + 2 * ec;

  u32 epoch, aux;
  {
    const u32 z = 0;
    asm volatile("global_load_dword %0, %1, %2 sc1" : "=v"(epoch) : "v"(z), "s"(ws) : "memory");
    const char* ap = isD ? b1 + fidx * 32 : isE ? b2 + cs * 32 : isC ? x + cs * 32 : x;
    const u32 ao = ecol * 2 + (isC ? eb * 1280 : 0);
    asm volatile("global_load_dword %0, %1, %2" : "=v"(aux) : "v"(ao), "s"(ap) : "memory");
  }
  if (w == 0) {
    const u32 rblk = isC ? WS_XP + (cs * 2 + kh) * 256 : isD ? WS_H + fidx * 256 : isE ? WS_X2 + cs * 256 : WS_DUMMY;
    const u32 rl = isC ? XP_L : isD ? H_L : 0;
    const u32 ro = g < 2 ? WS_O + g * O_L + h * 2048 + b * 128 + (n >> 3) * 1024 + (n & 7) * 16 : rblk + (g - 2) * rl + n * 16;
    const v4u sv = {SENT, SENT, SENT, SENT};
    asm volatile("global_store_dwordx4 %0, %1, %2 sc1\n\ts_nop 1" ::"v"(ro), "v"(sv), "s"(ws) : "memory");
  }
  v4u B[5];
#pragma unroll
  for (int i = 0; i < 5; i++) B[i] = v4u{ONES, ONES, ONES, ONES};
  auto dmaW = [&](const char* base, u32 o, int cnt, int mod, int start, int step, u32 ldst) {
    int ii = start;
    for (int i = 0; i < cnt; i++) {
      const char* p = base + ii * step;
      const u32 m0v = ldst + ii * 1024;
      asm volatile("s_mov_b32 m0, %2\n\ts_nop 0\n\tglobal_load_lds_dwordx4 %0, %1 nt" ::"v"(o), "s"(p), "s"(m0v) : "memory");
      ii++; if (ii == mod) ii = 0;
    }
  };
  if (isA) {
    const u32 xo = (n & 7) * 1280 + g * 16;
    const char* xb = x + w * 320;
    u64 sv; const u64 mk = 0x00FF00FF00FF00FFull;
    asm volatile("s_mov_b64 %5, exec\n\ts_mov_b64 exec, %8\n\t"
                 "global_load_dwordx4 %0, %6, %7\n\tglobal_load_dwordx4 %1, %6, %7 offset:64\n\tglobal_load_dwordx4 %2, %6, %7 offset:128\n\t"
                 "global_load_dwordx4 %3, %6, %7 offset:192\n\tglobal_load_dwordx4 %4, %6, %7 offset:256\n\ts_mov_b64 exec, %5"
                 : "+v"(B[0]), "+v"(B[1]), "+v"(B[2]), "+v"(B[3]), "+v"(B[4]), "=&s"(sv) : "v"(xo), "s"(xb), "s"(mk) : "memory");
    dmaW(wq + (size_t)(w * 160) * 8192 + aidx * 64, (lane >> 2) * 8192 + (lane & 3) * 16, 10, 10, 5 * (r & 1), 16 * 8192, s.lbase);
    if (isD) dmaW(w1 + (size_t)(w * 160) * 3456 + fidx * 32, (lane >> 1) * 3456 + (lane & 1) * 16, 5, 5, sub, 32 * 3456, s.lbase + 10240);
  } else if (isC) {
    dmaW(wo + (size_t)(kh * 2048 + w * 512) * 1280 + cs * 32, (lane >> 1) * 1280 + (lane & 1) * 16, 16, 16, 4 * sub, 32 * 1280, s.lbase);
  } else if (isE) {
    const int cnt = w == 3 ? 12 : 14;
    dmaW(w2 + (size_t)(w * 448) * 1280 + cs * 32, (lane >> 1) * 1280 + (lane & 1) * 16, cnt, cnt, 3 * sub, 32 * 1280, s.lbase);
  }
#pragma unroll
  for (int j = 0; j < 4; j++) { const char* base = kb + j * 16384; LD4("=a", s.K, 4 * j, koff, base); }
  asm volatile("s_waitcnt vmcnt(16)" : "+v"(epoch), "+v"(aux), "+v"(B[0]), "+v"(B[1]), "+v"(B[2]), "+v"(B[3]), "+v"(B[4])::"memory");
  const u32 tag = __builtin_amdgcn_readfirstlane(epoch) + 1u;
  if (w == 0 && lane == 0) st_sc1(ws, WS_FLAGS + wg * 4, tag);
  v4u W[16];
  {
    const int q4 = (lane >> 2) & 3, p4 = lane & 3;
    if (isA) {
      const u32 a0 = s.lbase + (8 * g + q4) * 64 + 8 * p4, d0 = s.lbase + 10240 + (8 * g + q4) * 32 + 8 * p4;
#pragma unroll
      for (int i = 0; i < 10; i++) W[i] = tr_frag(a0 + (i >> 1) * 2048 + (i & 1) * 32, 256);
#pragma unroll
      for (int i = 0; i < 5; i++) W[10 + i] = tr_frag(d0 + i * 1024, 128);
      W[15] = v4u{ONES, ONES, ONES, ONES};
    } else {
      const u32 c0 = s.lbase + (32 * (p4 >> 1) + 8 * g + q4) * 32 + 8 * (p4 & 1);
#pragma unroll
      for (int i = 0; i < 16; i++) W[i] = tr_frag(c0 + (i >> 1) * 2048 + (i & 1) * 16, 128);
    }
  }
  asm volatile("s_waitcnt lgkmcnt(0)" ::: "memory");
  STAMP(1);
  f32x2 csD = {0.f, 0.f};
  float res_lo = bf_lo(aux), res_hi = bf_hi(aux);
  float xr_lo = 0.f, xr_hi = 0.f;

#pragma unroll 1
  for (int l = 0; l < 2; l++) {
    const bool first = l == 0;
    OPAQUE(ws);
    if (isA) {
      LANES();
      if (!first) {
#pragma unroll
        for (int i = 0; i < 5; i++) B[i] = v4u{ONES, ONES, ONES, ONES};
        const u32 xo = g * 128 + (n & 7) * 16;
        const char* xb = ws + WS_X2 + w * 2560;
        for (int it = 0; it < SPIN_CAP; it++) {
          u64 sv; const u64 mk = 0x00FF00FF00FF00FFull;
          asm volatile("s_mov_b64 %5, exec\n\ts_mov_b64 exec, %8\n\t"
                       "global_load_dwordx4 %0, %6, %7 sc1\n\tglobal_load_dwordx4 %1, %6, %7 offset:512 sc1\n\tglobal_load_dwordx4 %2, %6, %7 offset:1024 sc1\n\t"
                       "global_load_dwordx4 %3, %6, %7 offset:1536 sc1\n\tglobal_load_dwordx4 %4, %6, %7 offset:2048 sc1\n\ts_mov_b64 exec, %5\n\ts_waitcnt vmcnt(0)"
                       : "+v"(B[0]), "+v"(B[1]), "+v"(B[2]), "+v"(B[3]), "+v"(B[4]), "=&s"(sv) : "v"(xo), "s"(xb), "s"(mk) : "memory");
          const u32 mx = max(max(max(umax4(B[0]), umax4(B[1])), max(umax4(B[2]), umax4(B[3]))), umax4(B[4]));
          if (!__any(mx == SENT)) break;
        }
        STAMP(9);
      }
      f32x4 t0, t1, gr;
      asm volatile("s_nop 1\n\t" MF "%0, %3, %13, 0\n\t" MF "%1, %4, %13, 0\n\t" MF "%2, %13, %13, 0\n\t"
                   MF "%0, %5, %14, %0\n\t" MF "%1, %6, %14, %1\n\t" MF "%2, %14, %14, %2\n\t"
                   MF "%0, %7, %15, %0\n\t" MF "%1, %8, %15, %1\n\t" MF "%2, %15, %15, %2\n\t"
                   MF "%0, %9, %16, %0\n\t" MF "%1, %10, %16, %1\n\t" MF "%2, %16, %16, %2\n\t"
                   MF "%0, %11, %17, %0\n\t" MF "%1, %12, %17, %1\n\t" MF "%2, %17, %17, %2\n\ts_nop 7"
                   : "=&v"(t0), "=&v"(t1), "=&v"(gr)
                   : "v"(W[0]), "v"(W[1]), "v"(W[2]), "v"(W[3]), "v"(W[4]), "v"(W[5]), "v"(W[6]), "v"(W[7]), "v"(W[8]), "v"(W[9]),
                     "v"(B[0]), "v"(B[1]), "v"(B[2]), "v"(B[3]), "v"(B[4]));
      if (n <= 8) {
        const u32 a = n < 8 ? lds0 + SC_ROLE + w * 1024 + (g >> 1) * 256 + n * 32 + (g & 1) * 16 : lds0 + SC_CS + w * 128 + g * 16;
        lds_st<f32x4>(a, t0);
        lds_st<f32x4>(a + (n < 8 ? 512 : 64), t1);
      }
      if (n < 8 && (g == 2 || g == (n >> 2))) {
        const float gv = g == 2 ? gr[0] : (n & 3) == 0 ? gr[0] : (n & 3) == 1 ? gr[1] : (n & 3) == 2 ? gr[2] : gr[3];
        lds_st<float>(lds0 + SC_STAT + w * 64 + n * 8 + (g == 2 ? 4 : 0), gv);
      }
      if (first) {
        f32x4 c1;
        asm volatile("s_nop 1\n\t" MF "%0, %1, %6, 0\n\t" MF "%0, %2, %6, %0\n\t" MF "%0, %3, %6, %0\n\t" MF "%0, %4, %6, %0\n\t" MF "%0, %5, %6, %0\n\ts_nop 7"
                     : "=&v"(c1) : "v"(W[10]), "v"(W[11]), "v"(W[12]), "v"(W[13]), "v"(W[14]), "v"(W[15]));
        if (n == 0) lds_st<f32x4>(lds0 + SC_CS + 512 + w * 64 + g * 16, c1);
      }
      lds_barrier();
      if (w < 2) {
        f32x2 acc = {0.f, 0.f}, csum = {0.f, 0.f}, st = {0.f, 0.f};
#pragma unroll
        for (int ww = 0; ww < 4; ww++) {
          acc += lds_ld<f32x2>(lds0 + SC_ROLE + ww * 1024 + w * 512 + lane * 8);
          csum += lds_ld<f32x2>(lds0 + SC_CS + ww * 128 + w * 64 + ecol * 4);
          st += lds_ld<f32x2>(lds0 + SC_STAT + ww * 64 + eb * 8);
        }
        if (first) {
#pragma unroll
          for (int ww = 0; ww < 4; ww++) csD += lds_ld<f32x2>(lds0 + SC_CS + 512 + ww * 64 + ecol * 4);
        }
        const float mean = st[1] * (1.f / 640.f), var = fmaxf(st[0] * (1.f / 640.f) - mean * mean, 0.f);
        const float rs = __builtin_amdgcn_rsqf(var + 1e-5f);
        const v2u gq = {pk_bf16(rs * (acc[0] - mean * csum[0]), rs * (acc[1] - mean * csum[1])), tag};
        st2_sc1(ws + WS_Q + l * Q_L, (eb * 128 + aidx) * 128 + (8 * w + 4 * eh + ec) * 8, gq);
      }
      if (!first) STAMP(10);
    }
    LANES();
    s.voff = lane * 16; s.vrun = s.voff; s.skip = l;
    OPAQUE(s.vb); OPAQUE(s.lbase);
    const u32 qoff = (4 * g + (n >> 2)) * 32;
    const char* qb = ws + WS_Q + l * Q_L + (b * 128 + h * 4) * 128;
    v4u qg0, qg1;
    bool ok = false;
    if (first) {
      const u32 koff2 = (n >> 2) * 256 + g * 64 + (n & 3) * 16;
      OPAQUE(kb);
#pragma unroll
      for (int j = 4; j < 16; j++) { const char* base = kb + j * 16384; LD4("+a", s.K, 4 * j, koff2, base); }
      v4u fl;
      const char* fb = ws + WS_FLAGS;
      asm volatile("global_load_dwordx4 %0, %3, %4 sc1\n\tglobal_load_dwordx4 %1, %3, %4 offset:16 sc1\n\tglobal_load_dwordx4 %2, %5, %6 sc1"
                   : "=&v"(qg0), "=&v"(qg1), "=&v"(fl) : "v"(qoff), "s"(qb), "v"(s.voff), "s"(fb) : "memory");
      issue_v<0>(s); issue_v<1>(s);
      asm volatile("s_waitcnt vmcnt(8)" : "+v"(qg0), "+v"(qg1), "+v"(fl)::"memory");
#pragma unroll 1
      for (int it = 0; it < SPIN_CAP; it++) {
        if (__all(fl[0] == tag && fl[1] == tag && fl[2] == tag && fl[3] == tag)) break;
        asm volatile("global_load_dwordx4 %0, %1, %2 sc1\n\ts_waitcnt vmcnt(0)" : "=&v"(fl) : "v"(s.voff), "s"(fb) : "memory");
      }
      ok = __all(qg0[1] == tag && qg0[3] == tag && qg1[1] == tag && qg1[3] == tag);
    } else asm volatile("" : "=v"(qg0), "=v"(qg1));
#pragma unroll 1
    for (int it = 0; !ok && it < SPIN_CAP; it++) {
      asm volatile("global_load_dwordx4 %0, %2, %3 sc1\n\tglobal_load_dwordx4 %1, %2, %3 offset:16 sc1\n\ts_waitcnt vmcnt(0)"
                   : "=&v"(qg0), "=&v"(qg1) : "v"(qoff), "s"(qb) : "memory");
      ok = __all(qg0[1] == tag && qg0[3] == tag && qg1[1] == tag && qg1[3] == tag);
    }
    if (first) STAMP(2); else STAMP(11);
    float res[4] = {0.f, 0.f, 0.f, 0.f};
    {
      v4u bq[4];
#pragma unroll
      for (int u = 0; u < 4; u++) { const bool on = (n & 3) == u; bq[u] = v4u{on ? qg0[0] : 0u, on ? qg0[2] : 0u, on ? qg1[0] : 0u, on ? qg1[2] : 0u}; }
      asm volatile("s_nop 1" : "+v"(bq[0]), "+v"(bq[1]), "+v"(bq[2]), "+v"(bq[3]), "+v"(res[0]), "+v"(res[1]), "+v"(res[2]), "+v"(res[3]));
      f32x4 d[2][2];
      issue_v<2>(s);
      qk_period<0>(s, bq, d, res); issue_v<3>(s);
      qk_period<1>(s, bq, d, res); issue_v<4>(s);
      qk_period<2>(s, bq, d, res); issue_v<5>(s);
      qk_period<3>(s, bq, d, res); issue_v<6>(s);
      qk_period<4>(s, bq, d, res); issue_v<7>(s);
      qk_period<5>(s, bq, d, res); issue_v<8>(s);
      qk_period<6>(s, bq, d, res); issue_v<9>(s);
      qk_period<7>(s, bq, d, res);
      qk_period<8>(s, bq, d, res);
    }
    Pv p;
    VMWAIT(32);
#pragma unroll
    for (int i = 0; i < 8; i++) p.t[i >> 2][i & 3] = lds_ld<v4u>(s.lbase + s.voff + i * 1024);
    constexpr float C2 = 0.08838834764831845f * 1.4426950408889634f;
    const float mw = wave_max(fmaxf(fmaxf(res[0], res[1]), fmaxf(res[2], res[3]))) * C2;
    float pr[4];
#pragma unroll
    for (int z = 0; z < 4; z++) pr[z] = __builtin_amdgcn_exp2f(__builtin_fmaf(res[z], C2, -mw));
    p.X[0] = pk_bf16(pr[0], pr[1]); p.X[1] = pk_bf16(pr[2], pr[3]);
    const float lw = wave_sum((pr[0] + pr[1]) + (pr[2] + pr[3]));
#pragma unroll
    for (int rr = 0; rr < 4; rr++) {
      const u32 src = n < 8 ? 0x0100u : 0x0302u;
      p.sel[rr] = ((n & 7) >> 1) != rr ? 0x0c0c0c0cu : (n & 1) ? (src << 16) | 0x0c0cu : 0x0c0c0000u | src;
    }
    p.d1 = f32x4{0.f, 0.f, 0.f, 0.f}; p.d2 = p.d1;
    p.xb = bcast<0>(p.X[0]);
#pragma unroll
    for (int rr = 0; rr < 4; rr++) p.a[0][rr] = __builtin_amdgcn_perm(p.xb, p.xb, p.sel[rr]);
    p.xb = bcast<0>(p.X[1]);
    p.a[1][0] = __builtin_amdgcn_perm(p.xb, p.xb, p.sel[0]);
    asm volatile("s_nop 1" : "+v"(p.a[0]), "+v"(p.d1), "+v"(p.d2));
    GAP();
    pv_step<0>(s, p); pv_step<1>(s, p); pv_step<2>(s, p); pv_step<3>(s, p);
    pv_step<4>(s, p); pv_step<5>(s, p); pv_step<6>(s, p); pv_step<7>(s, p);
    pv_step<8>(s, p); pv_step<9>(s, p); pv_step<10>(s, p); pv_step<11>(s, p);
    pv_step<12>(s, p); pv_step<13>(s, p); pv_step<14>(s, p); pv_step<15>(s, p);
    asm volatile("s_nop 7" : "+v"(p.d1), "+v"(p.d2));
    if (first) STAMP(3); else STAMP(12);
    {
    LANES();
    lds_st<f32x4>(lds0 + SC_ATT + (w * 2 + (g >> 1)) * 512 + n * 32 + (g & 1) * 16, g < 2 ? p.d1 : p.d2);
    if (lane == 0) { lds_st<float>(lds0 + SC_ML + w * 4, mw); lds_st<float>(lds0 + SC_ML + 16 + w * 4, lw); }
    lds_barrier();
    if (w == 0) {
      const f32x4 m4 = lds_ld<f32x4>(lds0 + SC_ML), l4 = lds_ld<f32x4>(lds0 + SC_ML + 16);
      const float M = fmaxf(fmaxf(m4[0], m4[1]), fmaxf(m4[2], m4[3]));
      f32x2 o = {0.f, 0.f}; float L = 0.f;
#pragma unroll
      for (int ww = 0; ww < 4; ww++) {
        const float f = __builtin_amdgcn_exp2f(m4[ww] - M);
        L += f * l4[ww];
        o += f * (lds_ld<f32x2>(lds0 + SC_ATT + ww * 1024 + lane * 8) + lds_ld<f32x2>(lds0 + SC_ATT + ww * 1024 + 512 + lane * 8));
      }
      const float inv = __builtin_amdgcn_rcpf(L);
      st_sc1(ws + WS_O + l * O_L + h * 2048 + b * 128, eh * 1024 + (lane & 31) * 4, min(pk_bf16(o[0] * inv, o[1] * inv), SENT - 1));
    }
    }
    if (first) STAMP(4); else STAMP(13);
    if (isC || isE || isD) {
      LANES();
      OPAQUE(ws);
      if (isD) {
        v4u B[5];
        f32x4 t0, t1, t2;
        const char* xb = ws + WS_XP + l * XP_L + w * 5120 + 2048;
        const u32 xo = (g >> 1) * 512 + (n >> 3) * 256 + (g & 1) * 128 + (n & 7) * 16;
        for (int it = 0; it < SPIN_CAP; it++) {
          asm volatile("global_load_dwordx4 %0, %5, %6 offset:-2048 sc1\n\tglobal_load_dwordx4 %1, %5, %6 offset:-1024 sc1\n\t"
                       "global_load_dwordx4 %2, %5, %6 sc1\n\tglobal_load_dwordx4 %3, %5, %6 offset:1024 sc1\n\t"
                       "global_load_dwordx4 %4, %5, %6 offset:2048 sc1\n\ts_waitcnt vmcnt(0)"
                       : "=&v"(B[0]), "=&v"(B[1]), "=&v"(B[2]), "=&v"(B[3]), "=&v"(B[4]) : "v"(xo), "s"(xb) : "memory");
          const u32 mx = max(max(max(umax4(B[0]), umax4(B[1])), max(umax4(B[2]), umax4(B[3]))), umax4(B[4]));
          if (!__any(mx == SENT)) break;
        }
        if (first) STAMP(5); else STAMP(14);
        asm volatile("s_nop 1\n\t" MF "%0, %3, %9, 0\n\t" MF "%1, %9, %9, 0\n\t" MF "%2, %8, %9, 0\n\t"
                     MF "%0, %4, %10, %0\n\t" MF "%1, %10, %10, %1\n\t" MF "%2, %8, %10, %2\n\t"
                     MF "%0, %5, %11, %0\n\t" MF "%1, %11, %11, %1\n\t" MF "%2, %8, %11, %2\n\t"
                     MF "%0, %6, %12, %0\n\t" MF "%1, %12, %12, %1\n\t" MF "%2, %8, %12, %2\n\t"
                     MF "%0, %7, %13, %0\n\t" MF "%1, %13, %13, %1\n\t" MF "%2, %8, %13, %2\n\ts_nop 7"
                     : "=&v"(t0), "=&v"(t1), "=&v"(t2)
                     : "v"(W[10]), "v"(W[11]), "v"(W[12]), "v"(W[13]), "v"(W[14]), "v"(W[15]), "v"(B[0]), "v"(B[1]), "v"(B[2]), "v"(B[3]), "v"(B[4]));
        lds_st<f32x4>(lds0 + SC_ROLE + (w * 2 + (n >> 3)) * 512 + (g >> 1) * 256 + (n & 7) * 32 + (g & 1) * 16, t0);
        const bool isG = (g & 1) == ((n >> 2) & 1);
        const float gv = !isG ? t2[0] : (n & 3) == 0 ? t1[0] : (n & 3) == 1 ? t1[1] : (n & 3) == 2 ? t1[2] : t1[3];
        lds_st<float>(lds0 + SC_STAT + w * 256 + (n & 7) * 32 + (isG ? 0 : 16) + (g >> 1) * 8 + (n >> 3) * 4, gv);
      } else {
        if (isE && w == 0) {
          const char* xb = ws + WS_XP + l * XP_L + cs * 512;
          const u32 xo = lane * 4;
          u32 p1, p2;
          for (int it = 0; it < SPIN_CAP; it++) {
            asm volatile("global_load_dword %0, %2, %3 sc1\n\tglobal_load_dword %1, %2, %3 offset:256 sc1\n\ts_waitcnt vmcnt(0)"
                         : "=&v"(p1), "=&v"(p2) : "v"(xo), "s"(xb) : "memory");
            if (!__any(max(p1, p2) == SENT)) break;
          }
          xr_lo = bf_lo(p1) + bf_lo(p2); xr_hi = bf_hi(p1) + bf_hi(p2);
        }
        v4u B[8];
        f32x4 t0, t1;
        B[6] = v4u{0u, 0u, 0u, 0u}; B[7] = B[6];
        const int extra = __builtin_amdgcn_readfirstlane(isC ? 2 : (w == 3 ? 0 : 1));
        const char* gb = ws + (isC ? WS_O + l * O_L + (kh * 16 + 4 * w) * 2048 : WS_H + l * H_L + w * 7168) + 4096;
        const u32 go = isC ? (n & 7) * 128 + (n >> 3) * 64 + g * 16 : (n >> 3) * 512 + g * 128 + (n & 7) * 16;
        for (int it = 0; it < SPIN_CAP; it++) {
          asm volatile("global_load_dwordx4 %0, %8, %9 offset:-4096 sc1\n\tglobal_load_dwordx4 %1, %8, %9 offset:-3072 sc1\n\t"
                       "global_load_dwordx4 %2, %8, %9 offset:-2048 sc1\n\tglobal_load_dwordx4 %3, %8, %9 offset:-1024 sc1\n\t"
                       "global_load_dwordx4 %4, %8, %9 sc1\n\tglobal_load_dwordx4 %5, %8, %9 offset:1024 sc1\n\t"
                       "s_cmp_lt_i32 %10, 1\n\ts_cbranch_scc1 .Ly%=\n\tglobal_load_dwordx4 %6, %8, %9 offset:2048 sc1\n\t"
                       "s_cmp_lt_i32 %10, 2\n\ts_cbranch_scc1 .Ly%=\n\tglobal_load_dwordx4 %7, %8, %9 offset:3072 sc1\n.Ly%=:\n\ts_waitcnt vmcnt(0)"
                       : "=&v"(B[0]), "=&v"(B[1]), "=&v"(B[2]), "=&v"(B[3]), "=&v"(B[4]), "=&v"(B[5]), "+v"(B[6]), "+v"(B[7])
                       : "v"(go), "s"(gb), "s"(extra) : "memory", "scc");
          const u32 mx = max(max(max(umax4(B[0]), umax4(B[1])), max(umax4(B[2]), umax4(B[3]))), max(max(umax4(B[4]), umax4(B[5])), max(umax4(B[6]), umax4(B[7]))));
          if (!__any(mx == SENT)) break;
        }
        if (first) STAMP(5); else STAMP(14);
        asm volatile("s_nop 1\n\t" MF "%0, %2, %18, 0\n\t" MF "%1, %3, %18, 0\n\t" MF "%0, %4, %19, %0\n\t" MF "%1, %5, %19, %1\n\t"
                     MF "%0, %6, %20, %0\n\t" MF "%1, %7, %20, %1\n\t" MF "%0, %8, %21, %0\n\t" MF "%1, %9, %21, %1\n\t"
                     MF "%0, %10, %22, %0\n\t" MF "%1, %11, %22, %1\n\t" MF "%0, %12, %23, %0\n\t" MF "%1, %13, %23, %1\n\t"
                     "s_cmp_lt_i32 %26, 1\n\ts_cbranch_scc1 .Lx%=\n\t" MF "%0, %14, %24, %0\n\t" MF "%1, %15, %24, %1\n\t"
                     "s_cmp_lt_i32 %26, 2\n\ts_cbranch_scc1 .Lx%=\n\t" MF "%0, %16, %25, %0\n\t" MF "%1, %17, %25, %1\n.Lx%=:\n\ts_nop 7"
                     : "=&v"(t0), "=&v"(t1)
                     : "v"(W[0]), "v"(W[1]), "v"(W[2]), "v"(W[3]), "v"(W[4]), "v"(W[5]), "v"(W[6]), "v"(W[7]), "v"(W[8]), "v"(W[9]), "v"(W[10]), "v"(W[11]),
                       "v"(W[12]), "v"(W[13]), "v"(W[14]), "v"(W[15]),
                       "v"(B[0]), "v"(B[1]), "v"(B[2]), "v"(B[3]), "v"(B[4]), "v"(B[5]), "v"(B[6]), "v"(B[7]), "s"(extra) : "scc");
        if ((g >> 1) == (n >> 3)) {
          const u32 a = lds0 + SC_ROLE + (w * 2 + (n >> 3)) * 512 + (n & 7) * 32 + (g & 1) * 16;
          lds_st<f32x4>(a, t0);
          lds_st<f32x4>(a + 256, t1);
        }
      }
      lds_barrier();
      if (w == 0) {
        f32x2 acc = {0.f, 0.f};
#pragma unroll
        for (int i = 0; i < 8; i++) acc += lds_ld<f32x2>(lds0 + SC_ROLE + i * 512 + lane * 8);
        if (isD) {
          float ss = 0.f, sm = 0.f;
#pragma unroll
          for (int ww = 0; ww < 4; ww++) {
            const f32x4 gq = lds_ld<f32x4>(lds0 + SC_STAT + ww * 256 + eb * 32);
            const f32x2 rq = lds_ld<f32x2>(lds0 + SC_STAT + ww * 256 + eb * 32 + 16);
            ss += (gq[0] + gq[1]) + (gq[2] + gq[3]); sm += rq[0] + rq[1];
          }
          const float mean = sm * (1.f / 640.f), var = fmaxf(ss * (1.f / 640.f) - mean * mean, 0.f);
          const float r1 = __builtin_amdgcn_rsqf(var + 1e-5f);
          const float rs = r1 * __builtin_amdgcn_rsqf(var * r1 * r1 + 1e-5f);
          const float y0 = rs * (acc[0] - mean * csD[0]) + res_lo, y1 = rs * (acc[1] - mean * csD[1]) + res_hi;
          constexpr float GC = -2.f * 0.7978845608028654f * 1.4426950408889634f;
          const float e0 = __builtin_amdgcn_exp2f(GC * (y0 + 0.044715f * y0 * y0 * y0)), e1 = __builtin_amdgcn_exp2f(GC * (y1 + 0.044715f * y1 * y1 * y1));
          const u32 hv = pk_bf16(y0 * __builtin_amdgcn_rcpf(1.f + e0), y1 * __builtin_amdgcn_rcpf(1.f + e1));
          st_sc1(ws + WS_H + l * H_L + fidx * 256, lane * 4, min(hv, SENT - 1));
        } else if (isC) {
          const u32 xv = kh ? pk_bf16(acc[0], acc[1]) : pk_bf16(acc[0] + res_lo, acc[1] + res_hi);
          st_sc1(ws + WS_XP + l * XP_L + (cs * 2 + kh) * 256, lane * 4, min(xv, SENT - 1));
          if (first && kh == 0) {
            const char* xb = ws + WS_X2 + cs * 256;
            const u32 xo = lane * 4;
            u32 p1;
            for (int it = 0; it < SPIN_CAP; it++) {
              asm volatile("global_load_dword %0, %1, %2 sc1\n\ts_waitcnt vmcnt(0)" : "=&v"(p1) : "v"(xo), "s"(xb) : "memory");
              if (!__any(p1 == SENT)) break;
            }
            res_lo = bf_lo(p1); res_hi = bf_hi(p1);
          }
        } else {
          const u32 xv = pk_bf16(xr_lo + acc[0] + res_lo, xr_hi + acc[1] + res_hi);
          if (first) st_sc1(ws + WS_X2 + cs * 256, lane * 4, min(xv, SENT - 1));
          else {
            *(GAS u32*)(out + eb * 1280 + cs * 32 + ecol * 2) = xv;
            if (cs == 0 && lane == 0) st_sc1(ws, WS_EPOCH, tag);
          }
        }
      }
      if (first) STAMP(6); else STAMP(15);
    }
    asm volatile("" : "=v"(B[0]), "=v"(B[1]), "=v"(B[2]), "=v"(B[3]), "=v"(B[4]));
  }
}

__global__ void __launch_bounds__(64) hold_small(char* ws) {
  const u32 t0 = (u32)__builtin_amdgcn_s_memrealtime();
  while ((u32)__builtin_amdgcn_s_memrealtime() - t0 < 2000u) __builtin_amdgcn_s_sleep(1);
}
__global__ void __launch_bounds__(256, 1) hold_big(char* ws) {
  const u32 t0 = (u32)__builtin_amdgcn_s_memrealtime();
  asm volatile("" ::: "v255", "a255");
  while ((u32)__builtin_amdgcn_s_memrealtime() - t0 < 2000u) __builtin_amdgcn_s_sleep(1);
}
extern "C" void kernel_launch(void* const* d_in, const int* in_sizes, int n_in, void* d_out, int out_size, void* d_ws,
                              size_t ws_size, hipStream_t stream) {
  (void)hipFuncSetAttribute((const void*)mega, hipFuncAttributeMaxDynamicSharedMemorySize, LDS_BYTES);
  (void)hipFuncSetAttribute((const void*)hold_big, hipFuncAttributeMaxDynamicSharedMemorySize, LDS_BYTES);
  hold_big<<<256, 256, LDS_BYTES, stream>>>((char*)d_ws);
  mega<<<256, 256, LDS_BYTES, stream>>>((const char*)d_in[0], (const char*)d_in[1], (const char*)d_in[2], (const char*)d_in[3],
                                        (const char*)d_in[4], (const char*)d_in[6], (const char*)d_in[7], (const char*)d_in[8],
                                        (const char*)d_in[9], (char*)d_out, (char*)d_ws);
}
```

```cpp
#define KPOL "nt"
#define VPOL "nt"
#define WPOL ""
#include <hip/hip_runtime.h>
#include <stdint.h>

typedef unsigned u32;
typedef unsigned long long u64;
typedef u32 v4u __attribute__((ext_vector_type(4)));
typedef u32 v2u __attribute__((ext_vector_type(2)));
typedef float f32x4 __attribute__((ext_vector_type(4)));
typedef float f32x2 __attribute__((ext_vector_type(2)));
typedef short v4s __attribute__((ext_vector_type(4)));
typedef __bf16 bf16x2 __attribute__((ext_vector_type(2)));
#define LAS __attribute__((address_space(3)))
#define GAS __attribute__((address_space(1)))
#define DI __device__ __forceinline__
#define GAP() __builtin_amdgcn_sched_barrier(0)

#define POS_A 0
#define POS_CE 0
#define POS_D 8
#define PREFETCH 1
#define ALIGN 0
#define PACE 0
#define PACE_OFF 0
#define STR2(x) #x
#define STR(x) STR2(x)
#define PACE1 PACE
#define WSPREAD 1
#define VTOUCH 1
constexpr u32 CH = 16384, SIDE = 8192;
constexpr u32 WS_EPOCH = 0, WS_FLAGS = 0x400, WS_DUMMY = 0x800;
constexpr u32 WS_CAL = CH;
constexpr u32 WS_Q = WS_CAL + 4 * CH, Q_L = 16 * CH;
constexpr u32 WS_O = WS_Q + 2 * Q_L, O_L = 8 * CH;
constexpr u32 WS_XP = WS_O + 2 * O_L, XP_L = 4 * CH;
constexpr u32 WS_H = WS_XP + 2 * XP_L, H_L = 4 * CH;
constexpr u32 WS_X2 = WS_H + 2 * H_L;
constexpr u32 WS_STAMP = 0x180000;
#define NSTAMP 32
constexpr u32 LDS_WAVE = 36864, LDS_WST = 20480, LDS_SC = 4 * LDS_WAVE, SC_ATT = LDS_SC, SC_ML = SC_ATT + 4096, SC_ROLE = SC_ML + 256,
              SC_STAT = SC_ROLE + 4096, SC_CS = SC_STAT + 1024, SC_CSD = SC_CS + 512, LDS_BYTES = 163840;
constexpr u32 SENT = 0xFFFFFFFFu, ONES = 0x3F803F80u;
constexpr int SPIN_CAP = 1 << 17;

DI u32 pk_bf16(float lo, float hi) {
  f32x2 v = {lo, hi};
  return __builtin_bit_cast(u32, __builtin_convertvector(v, bf16x2));
}
DI float bf_lo(u32 x) { return __builtin_bit_cast(float, x << 16); }
DI float bf_hi(u32 x) { return __builtin_bit_cast(float, x & 0xffff0000u); }
DI u32 umax4(v4u a) { return max(max(a[0], a[1]), max(a[2], a[3])); }
template <int CTRL> DI float dppf(float x) {
  return __builtin_bit_cast(float, __builtin_amdgcn_mov_dpp(__builtin_bit_cast(int, x), CTRL, 0xf, 0xf, true));
}
DI float wave_sum(float x) {
  x += dppf<0xB1>(x); x += dppf<0x4E>(x); x += dppf<0x124>(x); x += dppf<0x128>(x);
  auto s = __builtin_amdgcn_permlane16_swap(__float_as_uint(x), __float_as_uint(x), false, false);
  x = __uint_as_float(s[0]) + __uint_as_float(s[1]);
  auto t = __builtin_amdgcn_permlane32_swap(__float_as_uint(x), __float_as_uint(x), false, false);
  return __uint_as_float(t[0]) + __uint_as_float(t[1]);
}
DI float wave_max4(float a, float b, float c, float d) {
  float x, y;
  asm volatile("v_max3_f32 %0, %2, %3, %4\n\tv_max_f32 %0, %0, %5\n\ts_nop 1\n\t"
               "v_max_f32_dpp %0, %0, %0 quad_perm:[1,0,3,2] row_mask:0xf bank_mask:0xf\n\ts_nop 1\n\t"
               "v_max_f32_dpp %0, %0, %0 quad_perm:[2,3,0,1] row_mask:0xf bank_mask:0xf\n\ts_nop 1\n\t"
               "v_max_f32_dpp %0, %0, %0 row_ror:4 row_mask:0xf bank_mask:0xf\n\ts_nop 1\n\t"
               "v_max_f32_dpp %0, %0, %0 row_ror:8 row_mask:0xf bank_mask:0xf\n\tv_mov_b32 %1, %0\n\ts_nop 1\n\t"
               "v_permlane16_swap_b32 %0, %1\n\tv_max_f32 %0, %0, %1\n\tv_mov_b32 %1, %0\n\ts_nop 1\n\t"
               "v_permlane32_swap_b32 %0, %1\n\tv_max_f32 %0, %0, %1"
               : "=&v"(x), "=&v"(y) : "v"(a), "v"(b), "v"(c), "v"(d));
  return x;
}
DI void lds_barrier() { asm volatile("s_waitcnt lgkmcnt(0)\n\ts_barrier" ::: "memory"); }
DI void st_sc1(char* base, u32 off, u32 v) { asm volatile("global_store_dword %0, %1, %2 sc1" ::"v"(off), "v"(v), "s"(base) : "memory"); }
DI void st2_sc1(char* base, u32 off, v2u v) { asm volatile("global_store_dwordx2 %0, %1, %2 sc1" ::"v"(off), "v"(v), "s"(base) : "memory"); }
template <typename T> DI T lds_ld(u32 a) { return *(const LAS T*)(size_t)a; }
template <typename T> DI void lds_st(u32 a, T v) { *(LAS T*)(size_t)a = v; }
DI v4u tr_frag(u32 a, u32 second) {
  const v4s lo = __builtin_amdgcn_ds_read_tr16_b64_v4i16((LAS v4s*)(size_t)a);
  const v4s hi = __builtin_amdgcn_ds_read_tr16_b64_v4i16((LAS v4s*)(size_t)(a + second));
  const v2u l = __builtin_bit_cast(v2u, lo), h = __builtin_bit_cast(v2u, hi);
  return v4u{l[0], l[1], h[0], h[1]};
}
DI int lane_id() { int l; asm volatile("v_mbcnt_lo_u32_b32 %0, -1, 0\n\tv_mbcnt_hi_u32_b32 %0, -1, %0" : "=v"(l)); return l; }
#define LANES() const int lane = lane_id(), g = lane >> 4, n = lane & 15, eh = lane >> 5, eb = (lane >> 2) & 7, ec = lane & 3, ecol = 8 * eh + 2 * ec; (void)g; (void)n; (void)eb; (void)ecol
#define OPAQUE(p) asm volatile("" : "+s"(p))
#define PIN(v) asm volatile("" : "+v"(v))
#define MF "v_mfma_f32_16x16x32_bf16 "
#define VMWAIT(n) asm volatile("s_waitcnt vmcnt(%0)" ::"i"(n) : "memory")

#define STAMP(i) do { } while (0)
#define CSTAMP(i) do { } while (0)

constexpr bool k_in_lds(int n) { return (n & 3) < 2 || n == 2; }
constexpr int k_slot(int n) { return n <= 2 ? n : (n >> 2) * 2 + 1 + (n & 3); }
constexpr int k_reg(int n) { return n == 3 ? 0 : (n >> 2) * 2 - 1 + (n & 1); }
struct St {
  v4u V[64], K[28];
  const char *kb, *vb;
  u32 lbase, tnext;
};
#define PACE_ASM(tn, t64, tlo, PACE_S) ".Lp%=:\n\ts_memrealtime " t64 "\n\ts_waitcnt lgkmcnt(0)\n\ts_sub_u32 " tlo ", " tlo ", " tn "\n\ts_cmp_lt_i32 " tlo ", 0\n\ts_cbranch_scc1 .Lp%=\n\ts_add_u32 " tn ", " tn ", " PACE_S "\n\t"
template <int DT> DI void pace(St& s) {
}
template <int N> DI void issue_k(St& s, u32 koff) {
  if constexpr (N != 3 && N != 6) pace<(N == 8 || N == 9 || N == 12 || N == 13) ? PACE : PACE1>(s);
  const char* base = s.kb + N * 16384;
  if constexpr (k_in_lds(N)) {
    asm volatile("s_add_u32 m0, %2, %3\n\ts_nop 0\n\tglobal_load_lds_dwordx4 %0, %1 " KPOL "\n\tglobal_load_lds_dwordx4 %0, %1 offset:1024 " KPOL "\n\t"
                 "global_load_lds_dwordx4 %0, %1 offset:2048 " KPOL "\n\tglobal_load_lds_dwordx4 %0, %1 offset:3072 " KPOL
                 ::"v"(koff), "s"(base), "s"(s.lbase), "i"(k_slot(N) * 4096) : "memory", "scc");
  } else {
    constexpr int r = 4 * k_reg(N);
    asm volatile("global_load_dwordx4 %0, %4, %5 " KPOL "\n\tglobal_load_dwordx4 %1, %4, %5 offset:1024 " KPOL "\n\t"
                 "global_load_dwordx4 %2, %4, %5 offset:2048 " KPOL "\n\tglobal_load_dwordx4 %3, %4, %5 offset:3072 " KPOL
                 : "+v"(s.K[r]), "+v"(s.K[r + 1]), "+v"(s.K[r + 2]), "+v"(s.K[r + 3]) : "v"(koff), "s"(base) : "memory");
  }
}
template <int J> DI void issue_v(St& s, u32& vrun, u32 lo, u32 cnt) {
  asm volatile("s_sub_u32 s96, %9, %7\n\ts_cmp_ge_u32 s96, %8\n\ts_cbranch_scc1 .Lv%=\n\t"
               "global_load_dwordx4 %0, %4, %6 " VPOL "\n\tglobal_load_dwordx4 %1, %4, %6 offset:1024 " VPOL "\n\t"
               "global_load_dwordx4 %2, %4, %6 offset:2048 " VPOL "\n\tglobal_load_dwordx4 %3, %4, %6 offset:3072 " VPOL "\n\tv_add_u32 %4, 0x4000, %4\n.Lv%=:"
               : "+a"(s.V[4 * J]), "+a"(s.V[4 * J + 1]), "+a"(s.V[4 * J + 2]), "+a"(s.V[4 * J + 3]), "+v"(vrun), "+s"(s.tnext)
               : "s"(s.vb), "s"(lo), "s"(cnt), "i"(J) : "memory", "scc", "s96", "s97");
}
template <int J0> DI void issue_v_range(St& s, u32& vrun, u32 lo, u32 hi) {
  const u32 cnt = hi - lo;
  if constexpr (J0 <= 0) issue_v<0>(s, vrun, lo, cnt);
  if constexpr (J0 <= 1) issue_v<1>(s, vrun, lo, cnt);
  issue_v<2>(s, vrun, lo, cnt); issue_v<3>(s, vrun, lo, cnt); issue_v<4>(s, vrun, lo, cnt); issue_v<5>(s, vrun, lo, cnt);
  issue_v<6>(s, vrun, lo, cnt); issue_v<7>(s, vrun, lo, cnt); issue_v<8>(s, vrun, lo, cnt); issue_v<9>(s, vrun, lo, cnt);
  issue_v<10>(s, vrun, lo, cnt); issue_v<11>(s, vrun, lo, cnt);
  issue_v<12>(s, vrun, lo, cnt); issue_v<13>(s, vrun, lo, cnt);
  issue_v<14>(s, vrun, lo, cnt); issue_v<15>(s, vrun, lo, cnt);
}

#define ROT0 "quad_perm:[0,1,2,3]"
#define ROT1 "row_ror:4"
#define ROT2 "row_ror:8"
#define ROT3 "row_ror:12"
#define FOLD(ra, rb, da, db, rot, bank) "v_add_f32_dpp " ra ", " da ", " ra " " rot " row_mask:0xf bank_mask:" bank "\n\tv_add_f32_dpp " rb ", " db ", " rb " " rot " row_mask:0xf bank_mask:" bank "\n\t"
constexpr int k_lds_off(int P, int i) { return k_slot(4 * (P & 3) + (i >> 1)) * 4096 + (2 * (P >> 2) + (i & 1)) * 1024; }
#define RDK(slot, off) "ds_read_b128 " slot ", %24 offset:" off "\n\t"
#define QKP(RD0, RD1, RD2, RD3, r0, r1, r2, r3, bank)                                                                                 \
  asm volatile("s_waitcnt lgkmcnt(2)\n\t" MF "%0, %4, %12, 0\n\t" RD0 MF "%1, %5, %12, 0\n\t" RD1 "s_waitcnt lgkmcnt(%c29)\n\t"         \
               MF "%0, %6, %13, %0\n\t" RD2 MF "%1, %7, %13, %1\n\t" RD3 MF "%0, %8, %14, %0\n\t"                                       \
               FOLD("%2", "%3", "%16", "%20", r0, bank) MF "%1, %9, %14, %1\n\t" FOLD("%2", "%3", "%17", "%21", r1, bank)              \
               MF "%0, %10, %15, %0\n\t" FOLD("%2", "%3", "%18", "%22", r2, bank) MF "%1, %11, %15, %1\n\t" FOLD("%2", "%3", "%19", "%23", r3, bank) \
               : "=&v"(d[c][0]), "=&v"(d[c][1]), "+v"(res[pz]), "+v"(res[pz + 1]), "+v"(ring[0]), "+v"(ring[1]), "+v"(ring[2]), "+v"(ring[3]) \
               : "v"(k2a), "v"(k2b), "v"(s.K[4 * k_reg(4 * sp + 3) + z]), "v"(s.K[4 * k_reg(4 * sp + 3) + z + 1]),                       \
                 "v"(bq[0]), "v"(bq[1]), "v"(bq[2]), "v"(bq[3]),                                                                        \
                 "v"(d[c ^ 1][0][0]), "v"(d[c ^ 1][0][1]), "v"(d[c ^ 1][0][2]), "v"(d[c ^ 1][0][3]),                                    \
                 "v"(d[c ^ 1][1][0]), "v"(d[c ^ 1][1][1]), "v"(d[c ^ 1][1][2]), "v"(d[c ^ 1][1][3]), "v"(la),                           \
                 "i"(k_lds_off(P + 1, 0)), "i"(k_lds_off(P + 1, 1)), "i"(k_lds_off(P + 1, 2)), "i"(k_lds_off(P + 1, 3)), "i"(P < 7 ? 2 : 0))
#define QKP2(r0, r1, r2, r3, bank) do { if constexpr (P < 7) QKP(RDK("%4", "%c25"), RDK("%5", "%c26"), RDK("%6", "%c27"), RDK("%7", "%c28"), r0, r1, r2, r3, bank); \
                                        else QKP("", "", "", "", r0, r1, r2, r3, bank); } while (0)
template <int P> DI void qk_period(St& s, const v4u (&bq)[4], f32x4 (&d)[2][2], float (&res)[4], v4u (&ring)[6], u32 la) {
  constexpr int z = 2 * (P >> 2), sp = P & 3, c = P & 1, pz = P > 0 ? 2 * ((P - 1) >> 2) : 0, ps = (P - 1) & 3;
  if constexpr (P == 0) {
    asm volatile(MF "%0, %6, %12, 0\n\tds_read_b128 %6, %14 offset:10240\n\t" MF "%1, %7, %12, 0\n\tds_read_b128 %7, %14 offset:11264\n\t"
                 MF "%0, %2, %10, %0\n\tds_read_b128 %2, %14 offset:%c15\n\t" MF "%1, %3, %10, %1\n\tds_read_b128 %3, %14 offset:%c16\n\t"
                 MF "%0, %4, %11, %0\n\tds_read_b128 %4, %14 offset:%c17\n\t" MF "%1, %5, %11, %1\n\tds_read_b128 %5, %14 offset:%c18\n\t"
                 MF "%0, %8, %13, %0\n\t" MF "%1, %9, %13, %1"
                 : "=&v"(d[0][0]), "=&v"(d[0][1]), "+v"(ring[0]), "+v"(ring[1]), "+v"(ring[2]), "+v"(ring[3]), "+v"(ring[4]), "+v"(ring[5])
                 : "v"(s.K[0]), "v"(s.K[1]), "v"(bq[0]), "v"(bq[1]), "v"(bq[2]), "v"(bq[3]), "v"(la),
                   "i"(k_lds_off(1, 0)), "i"(k_lds_off(1, 1)), "i"(k_lds_off(1, 2)), "i"(k_lds_off(1, 3)));
  } else if constexpr (P == 8) {
    asm volatile("s_nop 5\n\t" FOLD("%0", "%1", "%2", "%6", ROT3, "8") "s_nop 0\n\t" FOLD("%0", "%1", "%3", "%7", ROT2, "8") "s_nop 0\n\t"
                 FOLD("%0", "%1", "%4", "%8", ROT1, "8") "s_nop 0\n\t" FOLD("%0", "%1", "%5", "%9", ROT0, "8")
                 : "+v"(res[2]), "+v"(res[3])
                 : "v"(d[1][0][0]), "v"(d[1][0][1]), "v"(d[1][0][2]), "v"(d[1][0][3]), "v"(d[1][1][0]), "v"(d[1][1][1]), "v"(d[1][1][2]), "v"(d[1][1][3]));
  } else {
    const v4u& k2a = sp == 0 ? ring[4] : s.K[sp == 0 ? 0 : 4 * k_reg(4 * sp + 2) + z];
    const v4u& k2b = sp == 0 ? ring[5] : s.K[sp == 0 ? 0 : 4 * k_reg(4 * sp + 2) + z + 1];
    if constexpr (ps == 0) QKP2(ROT0, ROT3, ROT2, ROT1, "1");
    else if constexpr (ps == 1) QKP2(ROT1, ROT0, ROT3, ROT2, "2");
    else if constexpr (ps == 2) QKP2(ROT2, ROT1, ROT0, ROT3, "4");
    else QKP2(ROT3, ROT2, ROT1, ROT0, "8");
  }
}
template <int N> DI u32 bcast(u32 x) { return (u32)__builtin_amdgcn_mov_dpp((int)x, 0x150 + N, 0xf, 0xf, true); }
DI void mf_pv(f32x4& d, const v4u& a, const v4u& b) { asm volatile(MF "%0, %1, %2, %0" : "+v"(d) : "v"(a), "a"(b)); }

struct Pv { v4u a[2]; u32 xb; f32x4 d1, d2; u32 X[2], sel[4]; };
template <int J> DI void pv_step(St& s, Pv& p) {
  if constexpr (J >= 12) { VMWAIT(4 * (15 - J)); GAP(); }
  mf_pv(p.d1, p.a[0], s.V[4 * J]); GAP();
  p.a[1][1] = __builtin_amdgcn_perm(p.xb, p.xb, p.sel[1]); p.a[1][2] = __builtin_amdgcn_perm(p.xb, p.xb, p.sel[2]); p.a[1][3] = __builtin_amdgcn_perm(p.xb, p.xb, p.sel[3]);
  GAP();
  mf_pv(p.d2, p.a[0], s.V[4 * J + 1]); GAP();
  if constexpr (J < 15) { p.xb = bcast<(J + 1) & 15>(p.X[0]); p.a[0][0] = __builtin_amdgcn_perm(p.xb, p.xb, p.sel[0]); } else asm volatile("s_nop 0");
  GAP();
  mf_pv(p.d1, p.a[1], s.V[4 * J + 2]); GAP();
  if constexpr (J < 15) { p.a[0][1] = __builtin_amdgcn_perm(p.xb, p.xb, p.sel[1]); p.a[0][2] = __builtin_amdgcn_perm(p.xb, p.xb, p.sel[2]); p.a[0][3] = __builtin_amdgcn_perm(p.xb, p.xb, p.sel[3]); }
  GAP();
  mf_pv(p.d2, p.a[1], s.V[4 * J + 3]); GAP();
  if constexpr (J < 15) { p.xb = bcast<(J + 1) & 15>(p.X[1]); p.a[1][0] = __builtin_amdgcn_perm(p.xb, p.xb, p.sel[0]); }
  GAP();
}

#define RB0 "v[224:227]"
#define RB1 "v[228:231]"
#define RB2 "v[232:235]"
#define RB3 "v[236:239]"
#define RB4 "v[240:243]"
#define RB5 "v[244:247]"
#define RB6 "v[248:251]"
#define RB7 "v[252:255]"
#define RLD(BT, M, OFF) "s_mov_b64 exec, " M "\n\tglobal_load_dwordx4 " BT ", %[go], %[gb] offset:" OFF " sc1\n\t"
#define RSTEP(N, B0, BT, M, OFF, MFMAS) "s_waitcnt vmcnt(" N ")\n\tv_cmp_eq_u32_e64 " M ", -1, " B0 "\n\t" MFMAS RLD(BT, M, OFF) "s_mov_b64 exec, -1\n\t"
#define RTAIL(B0T, BTT, MT, OFFT, EXACT, REARM)                                                                                       \
  "s_cbranch_scc0 .Ldone%=\n.Lmiss%=:\n\ts_sub_u32 s98, s98, 1\n\ts_cbranch_scc1 .Lout%=\n\t"                                          \
  "s_cmp_eq_u64 " MT ", %[full]\n\ts_cbranch_scc0 .Lloop%=\n"                                                                          \
  ".Lspin%=:\n\ts_waitcnt vmcnt(0)\n\tv_cmp_eq_u32_e64 " MT ", -1, " B0T "\n\ts_nop 1\n\ts_cmp_eq_u64 " MT ", %[full]\n\ts_cbranch_scc0 .Lissue%=\n\t" \
  RLD(BTT, MT, OFFT) "s_mov_b64 exec, -1\n\ts_sub_u32 s98, s98, 1\n\ts_cbranch_scc0 .Lspin%=\n\ts_branch .Lout%=\n"                   \
  ".Lnan%=:\n\t" EXACT "v_cmp_eq_u32 vcc, -1, v223\n\ts_cbranch_vccz .Lout%=\n\ts_sub_u32 s98, s98, 1\n\ts_cbranch_scc1 .Lout%=\n\t"   \
  REARM "s_branch .Lissue%=\n"                                                                                                         \
  ".Ldone%=:\n\ts_nop 1\n\tv_cmp_u_f32 vcc, v216, v216\n\ts_cbranch_vccnz .Lnan%=\n.Lout%=:\n\ts_waitcnt vmcnt(0)"
#define XMAX(a, b, c, d) "v_max3_u32 v223, v223, " a ", " b "\n\tv_max3_u32 v223, v223, " c ", " d "\n\t"
#define EXACT5 "v_mov_b32 v223, 0\n\t" XMAX("v224", "v225", "v226", "v227") XMAX("v228", "v229", "v230", "v231") XMAX("v232", "v233", "v234", "v235") \
               XMAX("v236", "v237", "v238", "v239") XMAX("v240", "v241", "v242", "v243")
#define EXACT8 EXACT5 XMAX("v244", "v245", "v246", "v247") XMAX("v248", "v249", "v250", "v251") XMAX("v252", "v253", "v254", "v255")
#define ARM5 "s_mov_b64 s[80:81], %[full]\n\ts_mov_b64 s[82:83], %[full]\n\ts_mov_b64 s[84:85], %[full]\n\ts_mov_b64 s[86:87], %[full]\n\ts_mov_b64 s[88:89], %[full]\n\t"
#define ARM8 ARM5 "s_mov_b64 s[90:91], %[full]\n\ts_cmp_gt_u32 %[nf], 6\n\ts_cselect_b64 s[92:93], -1, 0\n\ts_cmp_gt_u32 %[nf], 7\n\ts_cselect_b64 s[94:95], -1, 0\n\t"
#define OR5 "s_or_b64 s[96:97], s[80:81], s[82:83]\n\ts_or_b64 vcc, s[84:85], s[86:87]\n\ts_or_b64 s[96:97], s[96:97], vcc\n\ts_or_b64 s[96:97], s[96:97], s[88:89]\n\t"
#define CLOB5 "v223", "v224", "v225", "v226", "v227", "v228", "v229", "v230", "v231", "v232", "v233", "v234", "v235", "v236", "v237", "v238", "v239", \
              "v240", "v241", "v242", "v243", "s80", "s81", "s82", "s83", "s84", "s85", "s86", "s87", "s88", "s89", "s96", "s97", "s98", "vcc", "scc", "memory"
#define CLOB8 CLOB5, "v244", "v245", "v246", "v247", "v248", "v249", "v250", "v251", "v252", "v253", "v254", "v255", "s90", "s91", "s92", "s93", "s94", "s95"
#define FILL4(a, b, c, d, v) "v_mov_b32 " a ", " v "\n\tv_mov_b32 " b ", " v "\n\tv_mov_b32 " c ", " v "\n\tv_mov_b32 " d ", " v "\n\t"
#define RING5(PRE, O0, O1, O2, O3, O4, MM0, MM1, MM2, MM3, MM4)                                                                        \
  PRE "s_mov_b32 s98, 0x20000\n\t" ARM5 ".Lissue%=:\n\t" RLD(RB0, "s[80:81]", O0) RLD(RB1, "s[82:83]", O1) RLD(RB2, "s[84:85]", O2)      \
  RLD(RB3, "s[86:87]", O3) RLD(RB4, "s[88:89]", O4) "s_mov_b64 exec, -1\n.Lloop%=:\n\t"                                                  \
  RSTEP("4", "v224", RB0, "s[80:81]", O0, MM0) RSTEP("4", "v228", RB1, "s[82:83]", O1, MM1) RSTEP("4", "v232", RB2, "s[84:85]", O2, MM2) \
  RSTEP("4", "v236", RB3, "s[86:87]", O3, MM3) RSTEP("4", "v240", RB4, "s[88:89]", O4, MM4) OR5                                         \
  RTAIL("v224", RB0, "s[80:81]", O0, EXACT5, ARM5)

DI u32 poll_dword(const char* base, u32 off) {
  u32 v;
#pragma unroll 1
  for (int it = 0; it < SPIN_CAP; it++) {
    asm volatile("global_load_dword %0, %1, %2 sc1\n\ts_waitcnt vmcnt(0)" : "=&v"(v) : "v"(off), "s"(base) : "memory");
    if (!__any(v == SENT)) break;
  }
  return v;
}
__global__ void __launch_bounds__(256, 1)
mega(const char* x, const char* kc, const char* vc, const char* wq, const char* wo, const char* w1, const char* b1,
     const char* w2, const char* b2, char* out, char* ws_raw) {
  extern __shared__ __attribute__((aligned(16))) char smem[];
  STAMP(0);
  char* ws = (char*)(((size_t)ws_raw + 0x1FFFFF) & ~(size_t)0x1FFFFF);
  int w_ = __builtin_amdgcn_readfirstlane(threadIdx.x >> 6), wg_ = blockIdx.x;
#define ROLES()                                                                                                             \
  OPAQUE(w_); OPAQUE(wg_);                                                                                                  \
  const int w = w_, wg = wg_, xg = wg & 7, r = wg >> 3, sub = r & 3, b = xg, h = r, grp = (r >> 2) * 4 + (xg & 3);          \
  const bool isA = xg >= 4, isC = !isA && grp < 20, isE = !isA && grp >= 20 && grp < 30, isD = isA && grp < 27;             \
  const int aidx = (xg & 3) * 32 + r, fidx = grp * 4 + sub, kh = grp >= 10 ? 1 : 0, cs = (isC ? grp - 10 * kh : grp - 20) * 4 + sub; \
  const u32 csoff = (u32)(cs / 10) * CH, csr = cs % 10;                                                                      \
  (void)b; (void)h; (void)isD; (void)isE; (void)aidx; (void)fidx; (void)csoff; (void)csr; (void)w
  ROLES();
  const u32 lds0 = (u32)(size_t)(LAS char*)smem;
  St s;
  s.lbase = lds0 + w * LDS_WAVE;
  const size_t kvoff = (size_t)(b * 32 + h) * 1048576 + 786432 + w * 4096;
  s.kb = kc + kvoff;
  s.vb = vc + kvoff;
  u32 tag, aux;
  v4u B[5], W[16];
  {
    LANES();
    const u32 koff = (n >> 2) * 256 + g * 64 + (n & 3) * 16;
    issue_k<3>(s, koff);
    issue_k<6>(s, koff);
    s.tnext = (u32)__builtin_amdgcn_s_memrealtime() + PACE_OFF;
    GAP();
    u32 pf = 0;
    if (w == 2) {
      u64 pc; u32 len;
      asm volatile("s_getpc_b64 %0\n.Lpc%=:\n\ts_mov_b32 %1, .Lmega_end-.Lpc%=" : "=s"(pc), "=s"(len));
      const u32 po = min((u32)(r * 8 + (lane & 7)) * 128u, len - 4u) & ~3u;
      asm volatile("global_load_dword %0, %1, %2" : "=v"(pf) : "v"(po), "s"(pc) : "memory");
    }
    if (w == 1) {
      const u32 po = (lane & (VTOUCH - 1)) * (262144 / VTOUCH);
      asm volatile("global_load_dword %0, %1, %2 offset:-4096 nt" : "=v"(pf) : "v"(po), "s"(s.vb) : "memory");
    }
    u32 epoch;
    {
      const u32 z = 0;
      asm volatile("global_load_dword %0, %1, %2 sc1" : "=v"(epoch) : "v"(z), "s"(ws) : "memory");
      const char* ap = isD ? b1 + fidx * 32 : isE ? b2 + cs * 32 : isC ? x + cs * 32 : x;
      const u32 ao = ecol * 2 + (isC ? eb * 1280 : 0);
      asm volatile("global_load_dword %0, %1, %2" : "=v"(aux) : "v"(ao), "s"(ap) : "memory");
    }
    const u32 own = WS_O + (h >> 2) * CH + (h & 3) * 2048 + b * 128;
    if (w == 3) {
      const u32 rblk = isC ? WS_XP + csoff + csr * 512 + kh * 256 : isD ? WS_H + (fidx / 28) * CH + (fidx % 28) * 256 : isE ? WS_X2 + csoff + csr * 256 : WS_DUMMY;
      const u32 rl = isC ? XP_L : isD ? H_L : 0;
      const u32 ro = g < 2 ? own + g * O_L + (n >> 3) * 1024 + (n & 7) * 16 : rblk + (g - 2) * rl + n * 16;
      const v4u sv = {SENT, SENT, SENT, SENT};
      asm volatile("global_store_dwordx4 %0, %1, %2 sc1\n\tglobal_store_dwordx4 %0, %1, %3 sc1\n\ts_nop 1" ::"v"(ro), "v"(sv), "s"(ws), "s"(ws + SIDE) : "memory");
      if (isE) st_sc1(out, eb * 1280 + cs * 32 + ecol * 2, 0u);
    }
#pragma unroll
    for (int i = 0; i < 5; i++) B[i] = v4u{ONES, ONES, ONES, ONES};
    if (isA) {
      const u32 xo = (n & 7) * 1280 + g * 16;
      const char* xb = x + w * 320;
      u64 sv; const u64 mk = 0x00FF00FF00FF00FFull;
      asm volatile("s_mov_b64 %5, exec\n\ts_mov_b64 exec, %8\n\t"
                   "global_load_dwordx4 %0, %6, %7\n\tglobal_load_dwordx4 %1, %6, %7 offset:64\n\tglobal_load_dwordx4 %2, %6, %7 offset:128\n\t"
                   "global_load_dwordx4 %3, %6, %7 offset:192\n\tglobal_load_dwordx4 %4, %6, %7 offset:256\n\ts_mov_b64 exec, %5"
                   : "+v"(B[0]), "+v"(B[1]), "+v"(B[2]), "+v"(B[3]), "+v"(B[4]), "=&s"(sv) : "v"(xo), "s"(xb), "s"(mk) : "memory");
    }
    const char* wb = isA ? wq + (size_t)(w * 160) * 8192 + aidx * 64 : isC ? wo + (size_t)(kh * 2048 + w * 512) * 1280 + cs * 32 : w2 + (size_t)(w * 448) * 1280 + cs * 32;
    u32 wo_ = isA ? (lane >> 2) * 8192 + (lane & 3) * 16 : (lane >> 1) * 1280 + (lane & 1) * 16;
    int wstep = isA ? 16 * 8192 : 32 * 1280, wn = isA ? 10 : isC ? 16 : isE ? (w == 3 ? 12 : 14) : 0, wrot = isA ? (r & 1) : sub;
    int wpos = 0, wmask = isA ? 1 : 3, wper = isA ? 3 : 2;
    u32 wl = s.lbase + LDS_WST;
    auto w_at = [&](int pos) {
      if (isA && (pos == 6 || pos == 7)) return;
      const int tgt = min(wpos + wper, wn);
#pragma unroll 1
      for (; wpos < tgt; wpos++) {
        int ii = (wpos & ~wmask) | ((wpos + wrot) & wmask);
        if ((wpos | wmask) >= wn) ii = wpos;
        const char* p = wb + ii * wstep;
        const u32 m0v = wl + ii * 1024;
        asm volatile("s_mov_b32 m0, %2\n\ts_nop 0\n\tglobal_load_lds_dwordx4 %0, %1 " WPOL ::"v"(wo_), "s"(p), "s"(m0v) : "memory");
      }
    };
    u32 p1 = 0, p2 = 0;
    if (w == 3) asm volatile("s_load_dword %0, %1, 0x40" : "=s"(p1) : "s"(ws) : "memory");
    w_at(0);
    issue_k<7>(s, koff); w_at(1);
    issue_k<10>(s, koff); w_at(2);
    issue_k<11>(s, koff);
    asm volatile("s_waitcnt vmcnt(12) lgkmcnt(0)" : "+v"(epoch), "+v"(aux), "+v"(pf), "+s"(p1)::"memory");
    STAMP(24);
    tag = (__builtin_amdgcn_readfirstlane(epoch) + 2u) & ~1u;
    if (w == 3) {
      const u32 o1 = own + (r & 1) * SIDE, o2 = own + SIDE - (r & 1) * SIDE;
      u64 t0, t1, t2;
      asm volatile("s_memtime %0\n\ts_load_dword %3, %4, %5\n\ts_waitcnt lgkmcnt(0)\n\ts_memtime %1\n\ts_load_dword %3, %4, %6\n\ts_waitcnt lgkmcnt(0)\n\t"
                   "s_memtime %2\n\ts_waitcnt lgkmcnt(0)"
                   : "=&s"(t0), "=&s"(t1), "=&s"(t2), "=&s"(p2) : "s"(ws), "s"(o1), "s"(o2) : "memory");
      const u32 d1 = (u32)t1 - (u32)t0, d2 = (u32)t2 - (u32)t1;
      const u32 vote = ((d1 < d2 ? (r & 1) : 1 - (r & 1)) ^ (xg >> 2)) & 1;
      if (lane == 0) st_sc1(ws, WS_FLAGS + wg * 4, tag | vote);
    }
    w_at(3);
    if (isA) {
      wb = w1 + (size_t)(w * 160) * 3456 + fidx * 32; wo_ = (lane >> 1) * 3456 + (lane & 1) * 16; wstep = 32 * 3456;
      wn = isD ? 5 : 0; wrot = sub; wl += 10240;
      wpos = 0; wmask = 3; wper = 2;
    }
    issue_k<14>(s, koff); w_at(6);
    issue_k<15>(s, koff); w_at(7);
    STAMP(25);
    const int q4 = (lane >> 2) & 3, p4 = lane & 3;
#pragma unroll
    for (int i = 0; i < 16; i++) asm volatile("" : "=v"(W[i]));
    if (isA) {
      asm volatile("s_waitcnt vmcnt(8)" : "+v"(B[0]), "+v"(B[1]), "+v"(B[2]), "+v"(B[3]), "+v"(B[4])::"memory");
      const u32 a0 = s.lbase + LDS_WST + (8 * g + q4) * 64 + 8 * p4;
#pragma unroll
      for (int i = 0; i < 10; i++) W[i] = tr_frag(a0 + (i >> 1) * 2048 + (i & 1) * 32, 256);
    }
    STAMP(1);
    float res_lo = bf_lo(aux), res_hi = bf_hi(aux);
    u32 side = 0;

#pragma unroll 1
    for (int l = 0; l < 2; l++) {
      const bool first = l == 0;
      OPAQUE(ws);
      ROLES();
      if (isA) {
        LANES();
        u32 wa = n < 8 ? lds0 + SC_ROLE + w * 1024 + (g >> 1) * 256 + n * 32 + (g & 1) * 16 : lds0 + SC_CS + w * 128 + g * 16;
        u32 sa = lds0 + SC_STAT + w * 64 + n * 8 + (g == 2 ? 4 : 0);
        u32 qo = (eb * 2 + (aidx >> 6)) * CH + (((eb >> 2) * SIDE) ^ side) + (aidx & 63) * 128 + (8 * w + 4 * eh + ec) * 8;
        PIN(wa); PIN(sa); PIN(qo);
        f32x4 t0, t1, gr;
        if (first) {
          asm volatile("s_nop 1\n\t" MF "%0, %3, %13, 0\n\t" MF "%1, %4, %13, 0\n\t" MF "%2, %13, %13, 0\n\t"
                       MF "%0, %5, %14, %0\n\t" MF "%1, %6, %14, %1\n\t" MF "%2, %14, %14, %2\n\t"
                       MF "%0, %7, %15, %0\n\t" MF "%1, %8, %15, %1\n\t" MF "%2, %15, %15, %2\n\t"
                       MF "%0, %9, %16, %0\n\t" MF "%1, %10, %16, %1\n\t" MF "%2, %16, %16, %2\n\t"
                       MF "%2, %17, %17, %2\n\t" MF "%0, %11, %17, %0\n\t" MF "%1, %12, %17, %1\n\ts_nop 7"
                       : "=&{v[216:219]}"(t0), "=&v"(t1), "=&v"(gr)
                       : "v"(W[0]), "v"(W[1]), "v"(W[2]), "v"(W[3]), "v"(W[4]), "v"(W[5]), "v"(W[6]), "v"(W[7]), "v"(W[8]), "v"(W[9]),
                         "v"(B[0]), "v"(B[1]), "v"(B[2]), "v"(B[3]), "v"(B[4]));
        } else {
          const u32 xo = g * 128 + (n & 7) * 16;
          const char* xb = ws + WS_X2 + w * CH + (SIDE ^ side);
          const u64 mk = 0x00FF00FF00FF00FFull;
#define AMM(B, WA, WB, C0, C1, C2) MF "%0, " WA ", " B ", " C0 "\n\t" MF "%2, " B ", " B ", " C2 "\n\t" MF "%1, " WB ", " B ", " C1 "\n\t"
          asm volatile(RING5("v_mov_b32 v223, 0x3f803f80\n\t" FILL4("v224", "v225", "v226", "v227", "v223") FILL4("v228", "v229", "v230", "v231", "v223")
                             FILL4("v232", "v233", "v234", "v235", "v223") FILL4("v236", "v237", "v238", "v239", "v223") FILL4("v240", "v241", "v242", "v243", "v223"),
                             "0", "512", "1024", "1536", "2048",
                             AMM(RB0, "%3", "%4", "0", "0", "0"), AMM(RB1, "%5", "%6", "%0", "%1", "%2"), AMM(RB2, "%7", "%8", "%0", "%1", "%2"),
                             AMM(RB3, "%9", "%10", "%0", "%1", "%2"), AMM(RB4, "%11", "%12", "%0", "%1", "%2"))
                       : "=&{v[216:219]}"(t0), "=&v"(t1), "=&v"(gr)
                       : "v"(W[0]), "v"(W[1]), "v"(W[2]), "v"(W[3]), "v"(W[4]), "v"(W[5]), "v"(W[6]), "v"(W[7]), "v"(W[8]), "v"(W[9]),
                         [go] "v"(xo), [gb] "s"(xb), [full] "s"(mk)
                       : CLOB5);
          STAMP(9);
        }
        if (n <= 8) {
          lds_st<f32x4>(wa, t0);
          lds_st<f32x4>(wa + (n < 8 ? 512 : 64), t1);
        }
        if (n < 8 && (g == 2 || g == (n >> 2))) {
          const float gv = g == 2 ? gr[0] : (n & 3) == 0 ? gr[0] : (n & 3) == 1 ? gr[1] : (n & 3) == 2 ? gr[2] : gr[3];
          lds_st<float>(sa, gv);
        }
        lds_barrier();
        if (w < 2) {
          f32x2 acc = {0.f, 0.f}, csum = {0.f, 0.f}, st = {0.f, 0.f};
#pragma unroll
          for (int ww = 0; ww < 4; ww++) {
            st += lds_ld<f32x2>(lds0 + SC_STAT + ww * 64 + eb * 8);
            acc += lds_ld<f32x2>(lds0 + SC_ROLE + ww * 1024 + w * 512 + lane * 8);
            csum += lds_ld<f32x2>(lds0 + SC_CS + ww * 128 + w * 64 + ecol * 4);
          }
          const float mean = st[1] * (1.f / 640.f), var = fmaxf(st[0] * (1.f / 640.f) - mean * mean, 0.f);
          const float rs = __builtin_amdgcn_rsqf(var + 1e-5f);
          const v2u gq = {pk_bf16(rs * (acc[0] - mean * csum[0]), rs * (acc[1] - mean * csum[1])), tag};
          st2_sc1(ws + WS_Q + l * Q_L, qo, gq);
        }
        if (!first) STAMP(10);
      }
      LANES();
      OPAQUE(s.lbase);
      const u32 qoff = (4 * g + (n >> 2)) * 32;
      const char* qb = ws + WS_Q + l * Q_L + (b * 2 + (h >> 4)) * CH + (((b >> 2) * SIDE) ^ side) + (h & 15) * 512;
      u32 la = s.lbase + lane * 16, vrun = lane * 16, vhi = 16;
      v4u qg0, qg1, ring[6];
      bool ok = false;
      if (first) {
        const u32 koff2 = (n >> 2) * 256 + g * 64 + (n & 3) * 16;
        OPAQUE(s.kb); OPAQUE(s.vb);
        STAMP(26);
        issue_k<0>(s, koff2); w_at(8);
        issue_k<1>(s, koff2); w_at(9);
        issue_k<2>(s, koff2); w_at(10);
        issue_k<4>(s, koff2);
        issue_k<5>(s, koff2);
        STAMP(27);
        VMWAIT(8);
        STAMP(28);
        const int q4 = (lane >> 2) & 3, p4 = lane & 3;
        if (isA) {
          const u32 d0 = s.lbase + LDS_WST + 10240 + (8 * g + q4) * 32 + 8 * p4;
#pragma unroll
          for (int i = 0; i < 5; i++) W[10 + i] = tr_frag(d0 + i * 1024, 128);
          W[15] = v4u{ONES, ONES, ONES, ONES};
          f32x4 c1;
          asm volatile("s_nop 1\n\t" MF "%0, %1, %6, 0\n\t" MF "%0, %2, %6, %0\n\t" MF "%0, %3, %6, %0\n\t" MF "%0, %4, %6, %0\n\t" MF "%0, %5, %6, %0\n\ts_nop 7"
                       : "=&v"(c1) : "v"(W[10]), "v"(W[11]), "v"(W[12]), "v"(W[13]), "v"(W[14]), "v"(W[15]));
          if (n == 0) lds_st<f32x4>(lds0 + SC_CSD + w * 64 + g * 16, c1);
        } else {
          const u32 c0 = s.lbase + LDS_WST + (32 * (p4 >> 1) + 8 * g + q4) * 32 + 8 * (p4 & 1);
#pragma unroll
          for (int i = 0; i < 16; i++) W[i] = tr_frag(c0 + (i >> 1) * 2048 + (i & 1) * 16, 128);
          if (!isC) { W[14] = v4u{0u, 0u, 0u, 0u}; W[15] = W[14]; if (w == 3) { W[12] = W[14]; W[13] = W[14]; } }
        }
        asm volatile("s_waitcnt lgkmcnt(0)" ::: "memory");
        issue_k<8>(s, koff2); issue_k<9>(s, koff2); issue_k<12>(s, koff2); issue_k<13>(s, koff2);
        asm volatile("global_load_dwordx4 %0, %2, %3 sc1\n\tglobal_load_dwordx4 %1, %2, %3 offset:16 sc1"
                     : "=&v"(qg0), "=&v"(qg1) : "v"(qoff), "s"(qb) : "memory");
        STAMP(29);
        u32 lo = 0;
        vhi = 2 + 2 * w;
#pragma unroll 1
        for (;;) {
          issue_v_range<0>(s, vrun, lo, vhi);
          asm volatile("s_waitcnt vmcnt(8)" : "+v"(qg0), "+v"(qg1)::"memory");
          ok = __all(qg0[1] == tag && qg0[3] == tag && qg1[1] == tag && qg1[3] == tag);
          if (ok || vhi == 16) break;
          lo = vhi; vhi = 16;
        }
      } else asm volatile("" : "=v"(qg0), "=v"(qg1));
      asm volatile("ds_read_b128 %0, %6\n\tds_read_b128 %1, %6 offset:1024\n\tds_read_b128 %2, %6 offset:4096\n\tds_read_b128 %3, %6 offset:5120\n\t"
                   "ds_read_b128 %4, %6 offset:8192\n\tds_read_b128 %5, %6 offset:9216\n\ts_waitcnt lgkmcnt(0)"
                   : "=&v"(ring[0]), "=&v"(ring[1]), "=&v"(ring[2]), "=&v"(ring[3]), "=&v"(ring[4]), "=&v"(ring[5]) : "v"(la) : "memory");
      Pv p;
#pragma unroll
      for (int rr = 0; rr < 4; rr++) {
        const u32 src = n < 8 ? 0x0100u : 0x0302u;
        p.sel[rr] = ((n & 7) >> 1) != rr ? 0x0c0c0c0cu : (n & 1) ? (src << 16) | 0x0c0cu : 0x0c0c0000u | src;
        PIN(p.sel[rr]);
      }
      float res[4] = {0.f, 0.f, 0.f, 0.f};
      PIN(res[0]); PIN(res[1]); PIN(res[2]); PIN(res[3]);
#pragma unroll 1
      for (int it = 0; !ok && it < SPIN_CAP; it++) {
        asm volatile("global_load_dwordx4 %0, %2, %3 sc1\n\tglobal_load_dwordx4 %1, %2, %3 offset:16 sc1\n\ts_waitcnt vmcnt(0)"
                     : "=&v"(qg0), "=&v"(qg1) : "v"(qoff), "s"(qb) : "memory");
        ok = __all(qg0[1] == tag && qg0[3] == tag && qg1[1] == tag && qg1[3] == tag);
      }
      if (first) STAMP(2); else STAMP(11);
      CSTAMP(16);
      {
        v4u bq[4];
#pragma unroll
        for (int u = 0; u < 4; u++) { const bool on = (n & 3) == u; bq[u] = v4u{on ? qg0[0] : 0u, on ? qg0[2] : 0u, on ? qg1[0] : 0u, on ? qg1[2] : 0u}; }
        asm volatile("s_nop 1" : "+v"(bq[0]), "+v"(bq[1]), "+v"(bq[2]), "+v"(bq[3]));
        f32x4 d[2][2];
        qk_period<0>(s, bq, d, res, ring, la); qk_period<1>(s, bq, d, res, ring, la); qk_period<2>(s, bq, d, res, ring, la);
        qk_period<3>(s, bq, d, res, ring, la); qk_period<4>(s, bq, d, res, ring, la); qk_period<5>(s, bq, d, res, ring, la);
        qk_period<6>(s, bq, d, res, ring, la); qk_period<7>(s, bq, d, res, ring, la); qk_period<8>(s, bq, d, res, ring, la);
      }
      CSTAMP(17);
      constexpr float C2 = 0.08838834764831845f * 1.4426950408889634f;
      const float mw = wave_max4(res[0], res[1], res[2], res[3]) * C2;
      float pr[4];
#pragma unroll
      for (int z = 0; z < 4; z++) pr[z] = __builtin_amdgcn_exp2f(__builtin_fmaf(res[z], C2, -mw));
      p.X[0] = pk_bf16(pr[0], pr[1]); p.X[1] = pk_bf16(pr[2], pr[3]);
      float lsum = (pr[0] + pr[1]) + (pr[2] + pr[3]);
      if (first) {
        v4u fl;
        const char* fb = ws + WS_FLAGS;
        const u32 fo = lane * 16;
        asm volatile("global_load_dwordx4 %0, %1, %2 sc1" : "=&v"(fl) : "v"(fo), "s"(fb) : "memory");
        issue_v_range<2>(s, vrun, vhi, 16);
        STAMP(30);
        if (vhi == 16) VMWAIT(0);
        asm volatile("s_waitcnt vmcnt(16)" : "+v"(fl)::"memory");
#pragma unroll 1
        for (int it = 0; it < SPIN_CAP; it++) {
          if (__all(((fl[0] ^ tag) | (fl[1] ^ tag) | (fl[2] ^ tag) | (fl[3] ^ tag)) < 2u)) break;
          asm volatile("global_load_dwordx4 %0, %1, %2 sc1\n\ts_waitcnt vmcnt(0)" : "=&v"(fl) : "v"(fo), "s"(fb) : "memory");
        }
        const int votes = __popcll(__ballot(fl[0] & 1)) + __popcll(__ballot(fl[1] & 1)) + __popcll(__ballot(fl[2] & 1)) + __popcll(__ballot(fl[3] & 1));
        side = votes > 128 ? SIDE : 0;
      }
      p.d1 = f32x4{0.f, 0.f, 0.f, 0.f}; p.d2 = p.d1;
      p.xb = bcast<0>(p.X[0]);
#pragma unroll
      for (int rr = 0; rr < 4; rr++) p.a[0][rr] = __builtin_amdgcn_perm(p.xb, p.xb, p.sel[rr]);
      p.xb = bcast<0>(p.X[1]);
      p.a[1][0] = __builtin_amdgcn_perm(p.xb, p.xb, p.sel[0]);
      asm volatile("s_nop 1" : "+v"(p.a[0]), "+v"(p.d1), "+v"(p.d2));
      GAP();
      CSTAMP(18);
      pv_step<0>(s, p); pv_step<1>(s, p); pv_step<2>(s, p); pv_step<3>(s, p); pv_step<4>(s, p); pv_step<5>(s, p); pv_step<6>(s, p); pv_step<7>(s, p);
      pv_step<8>(s, p); pv_step<9>(s, p); pv_step<10>(s, p); pv_step<11>(s, p); pv_step<12>(s, p); pv_step<13>(s, p); pv_step<14>(s, p); pv_step<15>(s, p);
      {
        PIN(lsum);
        const float lw = wave_sum(lsum);
        LANES();
        ROLES();
        GAP();
        asm volatile("" : "+v"(p.d1), "+v"(p.d2));
        CSTAMP(19);
        if (first) STAMP(3); else STAMP(12);
        lds_st<f32x4>(lds0 + SC_ATT + (w * 2 + (g >> 1)) * 512 + n * 32 + (g & 1) * 16, g < 2 ? p.d1 : p.d2);
        if (lane == 0) lds_st<f32x2>(lds0 + SC_ML + w * 8, f32x2{mw, lw});
        lds_barrier();
        if (w == 0) {
          const f32x4 ma = lds_ld<f32x4>(lds0 + SC_ML), mb = lds_ld<f32x4>(lds0 + SC_ML + 16);
          f32x2 part[4];
#pragma unroll
          for (int ww = 0; ww < 4; ww++) part[ww] = lds_ld<f32x2>(lds0 + SC_ATT + ww * 1024 + lane * 8) + lds_ld<f32x2>(lds0 + SC_ATT + ww * 1024 + 512 + lane * 8);
          const float M = fmaxf(fmaxf(ma[0], ma[2]), fmaxf(mb[0], mb[2]));
          const float f0 = __builtin_amdgcn_exp2f(ma[0] - M), f1 = __builtin_amdgcn_exp2f(ma[2] - M), f2 = __builtin_amdgcn_exp2f(mb[0] - M), f3 = __builtin_amdgcn_exp2f(mb[2] - M);
          const float inv = __builtin_amdgcn_rcpf((f0 * ma[1] + f1 * ma[3]) + (f2 * mb[1] + f3 * mb[3]));
          const f32x2 o = (f0 * part[0] + f1 * part[1]) + (f2 * part[2] + f3 * part[3]);
          st_sc1(ws + WS_O + l * O_L + (h >> 2) * CH + side + (h & 3) * 2048 + b * 128, eh * 1024 + (lane & 31) * 4, min(pk_bf16(o[0] * inv, o[1] * inv), SENT - 1));
        }
        CSTAMP(20);
      }
      if (first) STAMP(4); else STAMP(13);
      {
      ROLES();
      if (isC || isE || isD) {
        LANES();
        OPAQUE(ws);
        char* pub = isD ? ws + WS_H + l * H_L + (fidx / 28) * CH + side + (fidx % 28) * 256
                        : isC ? ws + WS_XP + l * XP_L + csoff + (SIDE ^ side) + csr * 512 + kh * 256
                              : first ? ws + WS_X2 + csoff + (SIDE ^ side) + csr * 256 : out + cs * 32;
        u32 po = isE && !first ? eb * 1280 + ecol * 2 : lane * 4;
        OPAQUE(pub); PIN(po);
        float xr_lo = 0.f, xr_hi = 0.f;
        if (isD) {
          f32x4 t0, t1, t2;
          const char* xb = ws + WS_XP + l * XP_L + w * CH + (SIDE ^ side) + 2048;
          const u32 xo = (g >> 1) * 512 + (n >> 3) * 256 + (g & 1) * 128 + (n & 7) * 16;
          const bool isG = (g & 1) == ((n >> 2) & 1);
          u32 wa = lds0 + SC_ROLE + (w * 2 + (n >> 3)) * 512 + (g >> 1) * 256 + (n & 7) * 32 + (g & 1) * 16;
          u32 sa = lds0 + SC_STAT + w * 256 + (n & 7) * 32 + (isG ? 0 : 16) + (g >> 1) * 8 + (n >> 3) * 4;
          PIN(wa); PIN(sa);
          const u64 mk = ~0ull;
#define DMM(B, WA, C0, C1, C2) MF "%0, " WA ", " B ", " C0 "\n\t" MF "%1, " B ", " B ", " C1 "\n\t" MF "%2, %8, " B ", " C2 "\n\t"
          asm volatile(RING5("", "-2048", "-1024", "0", "1024", "2048", DMM(RB0, "%3", "0", "0", "0"), DMM(RB1, "%4", "%0", "%1", "%2"),
                             DMM(RB2, "%5", "%0", "%1", "%2"), DMM(RB3, "%6", "%0", "%1", "%2"), DMM(RB4, "%7", "%0", "%1", "%2"))
                       : "=&{v[216:219]}"(t0), "=&v"(t1), "=&v"(t2)
                       : "v"(W[10]), "v"(W[11]), "v"(W[12]), "v"(W[13]), "v"(W[14]), "v"(W[15]), [go] "v"(xo), [gb] "s"(xb), [full] "s"(mk)
                       : CLOB5);
          if (first) STAMP(5); else STAMP(14);
          lds_st<f32x4>(wa, t0);
          const float gv = !isG ? t2[0] : (n & 3) == 0 ? t1[0] : (n & 3) == 1 ? t1[1] : (n & 3) == 2 ? t1[2] : t1[3];
          lds_st<float>(sa, gv);
        } else {
          const char* gb = ws + (isC ? WS_O + l * O_L + (kh * 4 + w) * CH : WS_H + l * H_L + w * CH) + side + 4096;
          if (isE) {
            if (w == 0) {
              const char* xb = ws + WS_XP + l * XP_L + csoff + (SIDE ^ side) + csr * 512;
              const u32 p1 = poll_dword(xb, lane * 4), p2 = poll_dword(xb + 256, lane * 4);
              xr_lo = bf_lo(p1) + bf_lo(p2) + res_lo; xr_hi = bf_hi(p1) + bf_hi(p2) + res_hi;
            }
          }
          f32x4 t0, t1;
          const u32 go = isC ? (n & 7) * 128 + (n >> 3) * 64 + g * 16 : (n >> 3) * 512 + g * 128 + (n & 7) * 16;
          u32 wa = lds0 + SC_ROLE + (w * 2 + (n >> 3)) * 512 + (n & 7) * 32 + (g & 1) * 16;
          PIN(wa);
          const u64 mk = ~0ull;
          const int nf = __builtin_amdgcn_readfirstlane(isC ? 8 : w != 3 ? 7 : 6);
#define CMM(B, WA, WB, C0, C1) MF "%0, " WA ", " B ", " C0 "\n\t" MF "%1, " WB ", " B ", " C1 "\n\t"
          asm volatile(FILL4("v248", "v249", "v250", "v251", "0") FILL4("v252", "v253", "v254", "v255", "0") "s_mov_b32 s98, 0x20000\n\t" ARM8 ".Lissue%=:\n\t"
                       RLD(RB7, "s[94:95]", "3072") RLD(RB6, "s[92:93]", "2048") RLD(RB0, "s[80:81]", "-4096") RLD(RB1, "s[82:83]", "-3072")
                       RLD(RB2, "s[84:85]", "-2048") RLD(RB3, "s[86:87]", "-1024") RLD(RB4, "s[88:89]", "0") RLD(RB5, "s[90:91]", "1024")
                       "s_mov_b64 exec, -1\n.Lloop%=:\n\t"
                       RSTEP("7", "v252", RB7, "s[94:95]", "3072", CMM(RB7, "%16", "%17", "0", "0")) RSTEP("7", "v248", RB6, "s[92:93]", "2048", CMM(RB6, "%14", "%15", "%0", "%1"))
                       RSTEP("7", "v224", RB0, "s[80:81]", "-4096", CMM(RB0, "%2", "%3", "%0", "%1")) RSTEP("7", "v228", RB1, "s[82:83]", "-3072", CMM(RB1, "%4", "%5", "%0", "%1"))
                       RSTEP("7", "v232", RB2, "s[84:85]", "-2048", CMM(RB2, "%6", "%7", "%0", "%1")) RSTEP("7", "v236", RB3, "s[86:87]", "-1024", CMM(RB3, "%8", "%9", "%0", "%1"))
                       RSTEP("7", "v240", RB4, "s[88:89]", "0", CMM(RB4, "%10", "%11", "%0", "%1")) OR5
                       "s_or_b64 vcc, s[92:93], s[94:95]\n\ts_or_b64 s[96:97], s[96:97], vcc\n\t"
                       RSTEP("7", "v244", RB5, "s[90:91]", "1024", CMM(RB5, "%12", "%13", "%0", "%1"))
                       "s_or_b64 s[96:97], s[96:97], s[90:91]\n\ts_nop 1\n\t"
                       RTAIL("v224", RB0, "s[80:81]", "-4096", EXACT8, ARM8)
                       : "=&{v[216:219]}"(t0), "=&v"(t1)
                       : "v"(W[0]), "v"(W[1]), "v"(W[2]), "v"(W[3]), "v"(W[4]), "v"(W[5]), "v"(W[6]), "v"(W[7]), "v"(W[8]), "v"(W[9]), "v"(W[10]), "v"(W[11]),
                         "v"(W[12]), "v"(W[13]), "v"(W[14]), "v"(W[15]), [go] "v"(go), [gb] "s"(gb), [full] "s"(mk), [nf] "s"(nf)
                       : CLOB8);
          if (first) STAMP(5); else STAMP(14);
          if ((g >> 1) == (n >> 3)) {
            lds_st<f32x4>(wa, t0);
            lds_st<f32x4>(wa + 256, t1);
          }
        }
        lds_barrier();
        if (w == 0) {
          f32x2 pa[8];
#pragma unroll
          for (int i = 0; i < 8; i++) pa[i] = lds_ld<f32x2>(lds0 + SC_ROLE + i * 512 + lane * 8);
          if (isD) {
            f32x4 gq = {0.f, 0.f, 0.f, 0.f}; f32x2 rq = {0.f, 0.f}, csD = {0.f, 0.f};
#pragma unroll
            for (int ww = 0; ww < 4; ww++) {
              gq += lds_ld<f32x4>(lds0 + SC_STAT + ww * 256 + eb * 32);
              rq += lds_ld<f32x2>(lds0 + SC_STAT + ww * 256 + eb * 32 + 16);
              csD += lds_ld<f32x2>(lds0 + SC_CSD + ww * 64 + ecol * 4);
            }
            const f32x2 acc = ((pa[0] + pa[1]) + (pa[2] + pa[3])) + ((pa[4] + pa[5]) + (pa[6] + pa[7]));
            const float mean = (rq[0] + rq[1]) * (1.f / 640.f), var = fmaxf(((gq[0] + gq[1]) + (gq[2] + gq[3])) * (1.f / 640.f) - mean * mean, 0.f);
            const float rs = __builtin_amdgcn_rsqf(var + 1e-5f * (var + 1e-5f));
            const float y0 = rs * (acc[0] - mean * csD[0]) + res_lo, y1 = rs * (acc[1] - mean * csD[1]) + res_hi;
            constexpr float GC = -2.f * 0.7978845608028654f * 1.4426950408889634f;
            const float e0 = __builtin_amdgcn_exp2f(y0 * (GC + GC * 0.044715f * y0 * y0)), e1 = __builtin_amdgcn_exp2f(y1 * (GC + GC * 0.044715f * y1 * y1));
            const u32 hv = pk_bf16(y0 * __builtin_amdgcn_rcpf(1.f + e0), y1 * __builtin_amdgcn_rcpf(1.f + e1));
            st_sc1(pub, po, min(hv, SENT - 1));
            if (first) STAMP(6); else STAMP(15);
          } else {
            const f32x2 acc = ((pa[0] + pa[1]) + (pa[2] + pa[3])) + ((pa[4] + pa[5]) + (pa[6] + pa[7]));
            if (isC) {
              const u32 xv = kh ? pk_bf16(acc[0], acc[1]) : pk_bf16(acc[0] + res_lo, acc[1] + res_hi);
              st_sc1(pub, po, min(xv, SENT - 1));
              if (first) STAMP(6); else STAMP(15);
              if (first && kh == 0) {
                const u32 p1 = poll_dword(ws + WS_X2 + csoff + (SIDE ^ side) + csr * 256, lane * 4);
                res_lo = bf_lo(p1); res_hi = bf_hi(p1);
              }
            } else {
              const u32 xv = pk_bf16(xr_lo + acc[0], xr_hi + acc[1]);
              st_sc1(pub, po, min(xv, SENT - 1));
              if (!first && cs == 0 && lane == 0) st_sc1(ws, WS_EPOCH, tag);
              if (first) STAMP(6); else STAMP(15);
            }
          }
        }
      }
      }
      asm volatile("" : "=v"(B[0]), "=v"(B[1]), "=v"(B[2]), "=v"(B[3]), "=v"(B[4]), "=v"(wo_));
      asm volatile("" : "=s"(wb), "=s"(wstep), "=s"(wn), "=s"(wpos), "=s"(wrot), "=s"(wl), "=s"(s.kb), "=s"(s.vb), "=s"(s.tnext));
      asm volatile("" : "=s"(wmask), "=s"(wper));
    }
  }
  asm volatile(".Lmega_end:");
}

extern "C" void kernel_launch(void* const* d_in, const int* in_sizes, int n_in, void* d_out, int out_size, void* d_ws,
                              size_t ws_size, hipStream_t stream) {
  (void)hipFuncSetAttribute((const void*)mega, hipFuncAttributeMaxDynamicSharedMemorySize, LDS_BYTES);
  mega<<<256, 256, LDS_BYTES, stream>>>((const char*)d_in[0], (const char*)d_in[1], (const char*)d_in[2], (const char*)d_in[3],
                                        (const char*)d_in[4], (const char*)d_in[6], (const char*)d_in[7], (const char*)d_in[8],
                                        (const char*)d_in[9], (char*)d_out, (char*)d_ws);
}
```

```cpp
#define WAIT_AT 9
#define WAIT_US 0
#include <hip/hip_runtime.h>
#include <stdint.h>

typedef unsigned u32;
typedef unsigned long long u64;
typedef u32 v4u __attribute__((ext_vector_type(4)));
typedef u32 v2u __attribute__((ext_vector_type(2)));
typedef float f32x4 __attribute__((ext_vector_type(4)));
typedef float f32x2 __attribute__((ext_vector_type(2)));
typedef short v4s __attribute__((ext_vector_type(4)));
typedef __bf16 bf16x2 __attribute__((ext_vector_type(2)));
#define LAS __attribute__((address_space(3)))
#define GAS __attribute__((address_space(1)))
#define DI __device__ __forceinline__
#define GAP() __builtin_amdgcn_sched_barrier(0)

#define POS_A 0
#define POS_CE 0
#define POS_D 8
#define PREFETCH 1
#define ALIGN 0
#define PACE 0
#define PACE_OFF 0
#define STR2(x) #x
#define STR(x) STR2(x)
#define PACE1 PACE
#define WSPREAD 1
#define VTOUCH 1
#define KPOL "nt"
#define VPOL "nt"
#define WPOL ""
constexpr u32 CH = 16384, SIDE = 8192;
constexpr u32 WS_EPOCH = 0, WS_FLAGS = 0x400, WS_DUMMY = 0x800;
constexpr u32 WS_CAL = CH;
constexpr u32 WS_Q = WS_CAL + 4 * CH, Q_L = 16 * CH;
constexpr u32 WS_O = WS_Q + 2 * Q_L, O_L = 8 * CH;
constexpr u32 WS_XP = WS_O + 2 * O_L, XP_L = 4 * CH;
constexpr u32 WS_H = WS_XP + 2 * XP_L, H_L = 4 * CH;
constexpr u32 WS_X2 = WS_H + 2 * H_L;
constexpr u32 WS_STAMP = 0x180000;
#define NSTAMP 32
constexpr u32 LDS_WAVE = 36864, LDS_WST = 20480, LDS_SC = 4 * LDS_WAVE, SC_ATT = LDS_SC, SC_ML = SC_ATT + 4096, SC_ROLE = SC_ML + 256,
              SC_STAT = SC_ROLE + 4096, SC_CS = SC_STAT + 1024, SC_CSD = SC_CS + 512, LDS_BYTES = 163840;
constexpr u32 SENT = 0xFFFFFFFFu, ONES = 0x3F803F80u;
constexpr int SPIN_CAP = 1 << 17;

DI u32 pk_bf16(float lo, float hi) {
  f32x2 v = {lo, hi};
  return __builtin_bit_cast(u32, __builtin_convertvector(v, bf16x2));
}
DI float bf_lo(u32 x) { return __builtin_bit_cast(float, x << 16); }
DI float bf_hi(u32 x) { return __builtin_bit_cast(float, x & 0xffff0000u); }
DI u32 umax4(v4u a) { return max(max(a[0], a[1]), max(a[2], a[3])); }
template <int CTRL> DI float dppf(float x) {
  return __builtin_bit_cast(float, __builtin_amdgcn_mov_dpp(__builtin_bit_cast(int, x), CTRL, 0xf, 0xf, true));
}
DI float wave_sum(float x) {
  x += dppf<0xB1>(x); x += dppf<0x4E>(x); x += dppf<0x124>(x); x += dppf<0x128>(x);
  auto s = __builtin_amdgcn_permlane16_swap(__float_as_uint(x), __float_as_uint(x), false, false);
  x = __uint_as_float(s[0]) + __uint_as_float(s[1]);
  auto t = __builtin_amdgcn_permlane32_swap(__float_as_uint(x), __float_as_uint(x), false, false);
  return __uint_as_float(t[0]) + __uint_as_float(t[1]);
}
DI float wave_max4(float a, float b, float c, float d) {
  float x, y;
  asm volatile("v_max3_f32 %0, %2, %3, %4\n\tv_max_f32 %0, %0, %5\n\ts_nop 1\n\t"
               "v_max_f32_dpp %0, %0, %0 quad_perm:[1,0,3,2] row_mask:0xf bank_mask:0xf\n\ts_nop 1\n\t"
               "v_max_f32_dpp %0, %0, %0 quad_perm:[2,3,0,1] row_mask:0xf bank_mask:0xf\n\ts_nop 1\n\t"
               "v_max_f32_dpp %0, %0, %0 row_ror:4 row_mask:0xf bank_mask:0xf\n\ts_nop 1\n\t"
               "v_max_f32_dpp %0, %0, %0 row_ror:8 row_mask:0xf bank_mask:0xf\n\tv_mov_b32 %1, %0\n\ts_nop 1\n\t"
               "v_permlane16_swap_b32 %0, %1\n\tv_max_f32 %0, %0, %1\n\tv_mov_b32 %1, %0\n\ts_nop 1\n\t"
               "v_permlane32_swap_b32 %0, %1\n\tv_max_f32 %0, %0, %1"
               : "=&v"(x), "=&v"(y) : "v"(a), "v"(b), "v"(c), "v"(d));
  return x;
}
DI void lds_barrier() { asm volatile("s_waitcnt lgkmcnt(0)\n\ts_barrier" ::: "memory"); }
DI void st_sc1(char* base, u32 off, u32 v) { asm volatile("global_store_dword %0, %1, %2 sc1" ::"v"(off), "v"(v), "s"(base) : "memory"); }
DI void st2_sc1(char* base, u32 off, v2u v) { asm volatile("global_store_dwordx2 %0, %1, %2 sc1" ::"v"(off), "v"(v), "s"(base) : "memory"); }
template <typename T> DI T lds_ld(u32 a) { return *(const LAS T*)(size_t)a; }
template <typename T> DI void lds_st(u32 a, T v) { *(LAS T*)(size_t)a = v; }
DI v4u tr_frag(u32 a, u32 second) {
  const v4s lo = __builtin_amdgcn_ds_read_tr16_b64_v4i16((LAS v4s*)(size_t)a);
  const v4s hi = __builtin_amdgcn_ds_read_tr16_b64_v4i16((LAS v4s*)(size_t)(a + second));
  const v2u l = __builtin_bit_cast(v2u, lo), h = __builtin_bit_cast(v2u, hi);
  return v4u{l[0], l[1], h[0], h[1]};
}
DI int lane_id() { int l; asm volatile("v_mbcnt_lo_u32_b32 %0, -1, 0\n\tv_mbcnt_hi_u32_b32 %0, -1, %0" : "=v"(l)); return l; }
#define LANES() const int lane = lane_id(), g = lane >> 4, n = lane & 15, eh = lane >> 5, eb = (lane >> 2) & 7, ec = lane & 3, ecol = 8 * eh + 2 * ec; (void)g; (void)n; (void)eb; (void)ecol
#define OPAQUE(p) asm volatile("" : "+s"(p))
#define PIN(v) asm volatile("" : "+v"(v))
#define MF "v_mfma_f32_16x16x32_bf16 "
#define VMWAIT(n) asm volatile("s_waitcnt vmcnt(%0)" ::"i"(n) : "memory")

#define STAMP(i) do { } while (0)
#define CSTAMP(i) do { } while (0)

constexpr bool k_in_lds(int n) { return (n & 3) < 2 || n == 2; }
constexpr int k_slot(int n) { return n <= 2 ? n : (n >> 2) * 2 + 1 + (n & 3); }
constexpr int k_reg(int n) { return n == 3 ? 0 : (n >> 2) * 2 - 1 + (n & 1); }
struct St {
  v4u V[64], K[28];
  const char *kb, *vb;
  u32 lbase, tnext;
};
#define PACE_ASM(tn, t64, tlo, PACE_S) ".Lp%=:\n\ts_memrealtime " t64 "\n\ts_waitcnt lgkmcnt(0)\n\ts_sub_u32 " tlo ", " tlo ", " tn "\n\ts_cmp_lt_i32 " tlo ", 0\n\ts_cbranch_scc1 .Lp%=\n\ts_add_u32 " tn ", " tn ", " PACE_S "\n\t"
template <int DT> DI void pace(St& s) {
}
template <int N> DI void issue_k(St& s, u32 koff) {
  if constexpr (N != 3 && N != 6) pace<(N == 8 || N == 9 || N == 12 || N == 13) ? PACE : PACE1>(s);
  const char* base = s.kb + N * 16384;
  if constexpr (k_in_lds(N)) {
    asm volatile("s_add_u32 m0, %2, %3\n\ts_nop 0\n\tglobal_load_lds_dwordx4 %0, %1 " KPOL "\n\tglobal_load_lds_dwordx4 %0, %1 offset:1024 " KPOL "\n\t"
                 "global_load_lds_dwordx4 %0, %1 offset:2048 " KPOL "\n\tglobal_load_lds_dwordx4 %0, %1 offset:3072 " KPOL
                 ::"v"(koff), "s"(base), "s"(s.lbase), "i"(k_slot(N) * 4096) : "memory", "scc");
  } else {
    constexpr int r = 4 * k_reg(N);
    asm volatile("global_load_dwordx4 %0, %4, %5 " KPOL "\n\tglobal_load_dwordx4 %1, %4, %5 offset:1024 " KPOL "\n\t"
                 "global_load_dwordx4 %2, %4, %5 offset:2048 " KPOL "\n\tglobal_load_dwordx4 %3, %4, %5 offset:3072 " KPOL
                 : "+v"(s.K[r]), "+v"(s.K[r + 1]), "+v"(s.K[r + 2]), "+v"(s.K[r + 3]) : "v"(koff), "s"(base) : "memory");
  }
}
template <int J> DI void issue_v(St& s, u32& vrun, u32 lo, u32 cnt) {
  asm volatile("s_sub_u32 s96, %9, %7\n\ts_cmp_ge_u32 s96, %8\n\ts_cbranch_scc1 .Lv%=\n\t"
               "global_load_dwordx4 %0, %4, %6 " VPOL "\n\tglobal_load_dwordx4 %1, %4, %6 offset:1024 " VPOL "\n\t"
               "global_load_dwordx4 %2, %4, %6 offset:2048 " VPOL "\n\tglobal_load_dwordx4 %3, %4, %6 offset:3072 " VPOL "\n\tv_add_u32 %4, 0x4000, %4\n.Lv%=:"
               : "+a"(s.V[4 * J]), "+a"(s.V[4 * J + 1]), "+a"(s.V[4 * J + 2]), "+a"(s.V[4 * J + 3]), "+v"(vrun), "+s"(s.tnext)
               : "s"(s.vb), "s"(lo), "s"(cnt), "i"(J) : "memory", "scc", "s96", "s97");
}
template <int J0> DI void issue_v_range(St& s, u32& vrun, u32 lo, u32 hi) {
  const u32 cnt = hi - lo;
  if constexpr (J0 <= 0) issue_v<0>(s, vrun, lo, cnt);
  if constexpr (J0 <= 1) issue_v<1>(s, vrun, lo, cnt);
  issue_v<2>(s, vrun, lo, cnt); issue_v<3>(s, vrun, lo, cnt); issue_v<4>(s, vrun, lo, cnt); issue_v<5>(s, vrun, lo, cnt);
  issue_v<6>(s, vrun, lo, cnt); issue_v<7>(s, vrun, lo, cnt); issue_v<8>(s, vrun, lo, cnt); issue_v<9>(s, vrun, lo, cnt);
  issue_v<10>(s, vrun, lo, cnt); issue_v<11>(s, vrun, lo, cnt);
  issue_v<12>(s, vrun, lo, cnt); issue_v<13>(s, vrun, lo, cnt);
  issue_v<14>(s, vrun, lo, cnt); issue_v<15>(s, vrun, lo, cnt);
}

#define ROT0 "quad_perm:[0,1,2,3]"
#define ROT1 "row_ror:4"
#define ROT2 "row_ror:8"
#define ROT3 "row_ror:12"
#define FOLD(ra, rb, da, db, rot, bank) "v_add_f32_dpp " ra ", " da ", " ra " " rot " row_mask:0xf bank_mask:" bank "\n\tv_add_f32_dpp " rb ", " db ", " rb " " rot " row_mask:0xf bank_mask:" bank "\n\t"
constexpr int k_lds_off(int P, int i) { return k_slot(4 * (P & 3) + (i >> 1)) * 4096 + (2 * (P >> 2) + (i & 1)) * 1024; }
#define RDK(slot, off) "ds_read_b128 " slot ", %24 offset:" off "\n\t"
#define QKP(RD0, RD1, RD2, RD3, r0, r1, r2, r3, bank)                                                                                 \
  asm volatile("s_waitcnt lgkmcnt(2)\n\t" MF "%0, %4, %12, 0\n\t" RD0 MF "%1, %5, %12, 0\n\t" RD1 "s_waitcnt lgkmcnt(%c29)\n\t"         \
               MF "%0, %6, %13, %0\n\t" RD2 MF "%1, %7, %13, %1\n\t" RD3 MF "%0, %8, %14, %0\n\t"                                       \
               FOLD("%2", "%3", "%16", "%20", r0, bank) MF "%1, %9, %14, %1\n\t" FOLD("%2", "%3", "%17", "%21", r1, bank)              \
               MF "%0, %10, %15, %0\n\t" FOLD("%2", "%3", "%18", "%22", r2, bank) MF "%1, %11, %15, %1\n\t" FOLD("%2", "%3", "%19", "%23", r3, bank) \
               : "=&v"(d[c][0]), "=&v"(d[c][1]), "+v"(res[pz]), "+v"(res[pz + 1]), "+v"(ring[0]), "+v"(ring[1]), "+v"(ring[2]), "+v"(ring[3]) \
               : "v"(k2a), "v"(k2b), "v"(s.K[4 * k_reg(4 * sp + 3) + z]), "v"(s.K[4 * k_reg(4 * sp + 3) + z + 1]),                       \
                 "v"(bq[0]), "v"(bq[1]), "v"(bq[2]), "v"(bq[3]),                                                                        \
                 "v"(d[c ^ 1][0][0]), "v"(d[c ^ 1][0][1]), "v"(d[c ^ 1][0][2]), "v"(d[c ^ 1][0][3]),                                    \
                 "v"(d[c ^ 1][1][0]), "v"(d[c ^ 1][1][1]), "v"(d[c ^ 1][1][2]), "v"(d[c ^ 1][1][3]), "v"(la),                           \
                 "i"(k_lds_off(P + 1, 0)), "i"(k_lds_off(P + 1, 1)), "i"(k_lds_off(P + 1, 2)), "i"(k_lds_off(P + 1, 3)), "i"(P < 7 ? 2 : 0))
#define QKP2(r0, r1, r2, r3, bank) do { if constexpr (P < 7) QKP(RDK("%4", "%c25"), RDK("%5", "%c26"), RDK("%6", "%c27"), RDK("%7", "%c28"), r0, r1, r2, r3, bank); \
                                        else QKP("", "", "", "", r0, r1, r2, r3, bank); } while (0)
template <int P> DI void qk_period(St& s, const v4u (&bq)[4], f32x4 (&d)[2][2], float (&res)[4], v4u (&ring)[6], u32 la) {
  constexpr int z = 2 * (P >> 2), sp = P & 3, c = P & 1, pz = P > 0 ? 2 * ((P - 1) >> 2) : 0, ps = (P - 1) & 3;
  if constexpr (P == 0) {
    asm volatile(MF "%0, %6, %12, 0\n\tds_read_b128 %6, %14 offset:10240\n\t" MF "%1, %7, %12, 0\n\tds_read_b128 %7, %14 offset:11264\n\t"
                 MF "%0, %2, %10, %0\n\tds_read_b128 %2, %14 offset:%c15\n\t" MF "%1, %3, %10, %1\n\tds_read_b128 %3, %14 offset:%c16\n\t"
                 MF "%0, %4, %11, %0\n\tds_read_b128 %4, %14 offset:%c17\n\t" MF "%1, %5, %11, %1\n\tds_read_b128 %5, %14 offset:%c18\n\t"
                 MF "%0, %8, %13, %0\n\t" MF "%1, %9, %13, %1"
                 : "=&v"(d[0][0]), "=&v"(d[0][1]), "+v"(ring[0]), "+v"(ring[1]), "+v"(ring[2]), "+v"(ring[3]), "+v"(ring[4]), "+v"(ring[5])
                 : "v"(s.K[0]), "v"(s.K[1]), "v"(bq[0]), "v"(bq[1]), "v"(bq[2]), "v"(bq[3]), "v"(la),
                   "i"(k_lds_off(1, 0)), "i"(k_lds_off(1, 1)), "i"(k_lds_off(1, 2)), "i"(k_lds_off(1, 3)));
  } else if constexpr (P == 8) {
    asm volatile("s_nop 5\n\t" FOLD("%0", "%1", "%2", "%6", ROT3, "8") "s_nop 0\n\t" FOLD("%0", "%1", "%3", "%7", ROT2, "8") "s_nop 0\n\t"
                 FOLD("%0", "%1", "%4", "%8", ROT1, "8") "s_nop 0\n\t" FOLD("%0", "%1", "%5", "%9", ROT0, "8")
                 : "+v"(res[2]), "+v"(res[3])
                 : "v"(d[1][0][0]), "v"(d[1][0][1]), "v"(d[1][0][2]), "v"(d[1][0][3]), "v"(d[1][1][0]), "v"(d[1][1][1]), "v"(d[1][1][2]), "v"(d[1][1][3]));
  } else {
    const v4u& k2a = sp == 0 ? ring[4] : s.K[sp == 0 ? 0 : 4 * k_reg(4 * sp + 2) + z];
    const v4u& k2b = sp == 0 ? ring[5] : s.K[sp == 0 ? 0 : 4 * k_reg(4 * sp + 2) + z + 1];
    if constexpr (ps == 0) QKP2(ROT0, ROT3, ROT2, ROT1, "1");
    else if constexpr (ps == 1) QKP2(ROT1, ROT0, ROT3, ROT2, "2");
    else if constexpr (ps == 2) QKP2(ROT2, ROT1, ROT0, ROT3, "4");
    else QKP2(ROT3, ROT2, ROT1, ROT0, "8");
  }
}
template <int N> DI u32 bcast(u32 x) { return (u32)__builtin_amdgcn_mov_dpp((int)x, 0x150 + N, 0xf, 0xf, true); }
DI void mf_pv(f32x4& d, const v4u& a, const v4u& b) { asm volatile(MF "%0, %1, %2, %0" : "+v"(d) : "v"(a), "a"(b)); }

struct Pv { v4u a[2]; u32 xb; f32x4 d1, d2; u32 X[2], sel[4]; };
template <int J> DI void pv_step(St& s, Pv& p) {
  if constexpr (J >= 12) { VMWAIT(4 * (15 - J)); GAP(); }
  mf_pv(p.d1, p.a[0], s.V[4 * J]); GAP();
  p.a[1][1] = __builtin_amdgcn_perm(p.xb, p.xb, p.sel[1]); p.a[1][2] = __builtin_amdgcn_perm(p.xb, p.xb, p.sel[2]); p.a[1][3] = __builtin_amdgcn_perm(p.xb, p.xb, p.sel[3]);
  GAP();
  mf_pv(p.d2, p.a[0], s.V[4 * J + 1]); GAP();
  if constexpr (J < 15) { p.xb = bcast<(J + 1) & 15>(p.X[0]); p.a[0][0] = __builtin_amdgcn_perm(p.xb, p.xb, p.sel[0]); } else asm volatile("s_nop 0");
  GAP();
  mf_pv(p.d1, p.a[1], s.V[4 * J + 2]); GAP();
  if constexpr (J < 15) { p.a[0][1] = __builtin_amdgcn_perm(p.xb, p.xb, p.sel[1]); p.a[0][2] = __builtin_amdgcn_perm(p.xb, p.xb, p.sel[2]); p.a[0][3] = __builtin_amdgcn_perm(p.xb, p.xb, p.sel[3]); }
  GAP();
  mf_pv(p.d2, p.a[1], s.V[4 * J + 3]); GAP();
  if constexpr (J < 15) { p.xb = bcast<(J + 1) & 15>(p.X[1]); p.a[1][0] = __builtin_amdgcn_perm(p.xb, p.xb, p.sel[0]); }
  GAP();
}

#define RB0 "v[224:227]"
#define RB1 "v[228:231]"
#define RB2 "v[232:235]"
#define RB3 "v[236:239]"
#define RB4 "v[240:243]"
#define RB5 "v[244:247]"
#define RB6 "v[248:251]"
#define RB7 "v[252:255]"
#define RLD(BT, M, OFF) "s_mov_b64 exec, " M "\n\tglobal_load_dwordx4 " BT ", %[go], %[gb] offset:" OFF " sc1\n\t"
#define RSTEP(N, B0, BT, M, OFF, MFMAS) "s_waitcnt vmcnt(" N ")\n\tv_cmp_eq_u32_e64 " M ", -1, " B0 "\n\t" MFMAS RLD(BT, M, OFF) "s_mov_b64 exec, -1\n\t"
#define RTAIL(B0T, BTT, MT, OFFT, EXACT, REARM)                                                                                       \
  "s_cbranch_scc0 .Ldone%=\n.Lmiss%=:\n\ts_sub_u32 s98, s98, 1\n\ts_cbranch_scc1 .Lout%=\n\t"                                          \
  "s_cmp_eq_u64 " MT ", %[full]\n\ts_cbranch_scc0 .Lloop%=\n"                                                                          \
  ".Lspin%=:\n\ts_waitcnt vmcnt(0)\n\tv_cmp_eq_u32_e64 " MT ", -1, " B0T "\n\ts_nop 1\n\ts_cmp_eq_u64 " MT ", %[full]\n\ts_cbranch_scc0 .Lissue%=\n\t" \
  RLD(BTT, MT, OFFT) "s_mov_b64 exec, -1\n\ts_sub_u32 s98, s98, 1\n\ts_cbranch_scc0 .Lspin%=\n\ts_branch .Lout%=\n"                   \
  ".Lnan%=:\n\t" EXACT "v_cmp_eq_u32 vcc, -1, v223\n\ts_cbranch_vccz .Lout%=\n\ts_sub_u32 s98, s98, 1\n\ts_cbranch_scc1 .Lout%=\n\t"   \
  REARM "s_branch .Lissue%=\n"                                                                                                         \
  ".Ldone%=:\n\ts_nop 1\n\tv_cmp_u_f32 vcc, v216, v216\n\ts_cbranch_vccnz .Lnan%=\n.Lout%=:\n\ts_waitcnt vmcnt(0)"
#define XMAX(a, b, c, d) "v_max3_u32 v223, v223, " a ", " b "\n\tv_max3_u32 v223, v223, " c ", " d "\n\t"
#define EXACT5 "v_mov_b32 v223, 0\n\t" XMAX("v224", "v225", "v226", "v227") XMAX("v228", "v229", "v230", "v231") XMAX("v232", "v233", "v234", "v235") \
               XMAX("v236", "v237", "v238", "v239") XMAX("v240", "v241", "v242", "v243")
#define EXACT8 EXACT5 XMAX("v244", "v245", "v246", "v247") XMAX("v248", "v249", "v250", "v251") XMAX("v252", "v253", "v254", "v255")
#define ARM5 "s_mov_b64 s[80:81], %[full]\n\ts_mov_b64 s[82:83], %[full]\n\ts_mov_b64 s[84:85], %[full]\n\ts_mov_b64 s[86:87], %[full]\n\ts_mov_b64 s[88:89], %[full]\n\t"
#define ARM8 ARM5 "s_mov_b64 s[90:91], %[full]\n\ts_cmp_gt_u32 %[nf], 6\n\ts_cselect_b64 s[92:93], -1, 0\n\ts_cmp_gt_u32 %[nf], 7\n\ts_cselect_b64 s[94:95], -1, 0\n\t"
#define OR5 "s_or_b64 s[96:97], s[80:81], s[82:83]\n\ts_or_b64 vcc, s[84:85], s[86:87]\n\ts_or_b64 s[96:97], s[96:97], vcc\n\ts_or_b64 s[96:97], s[96:97], s[88:89]\n\t"
#define CLOB5 "v223", "v224", "v225", "v226", "v227", "v228", "v229", "v230", "v231", "v232", "v233", "v234", "v235", "v236", "v237", "v238", "v239", \
              "v240", "v241", "v242", "v243", "s80", "s81", "s82", "s83", "s84", "s85", "s86", "s87", "s88", "s89", "s96", "s97", "s98", "vcc", "scc", "memory"
#define CLOB8 CLOB5, "v244", "v245", "v246", "v247", "v248", "v249", "v250", "v251", "v252", "v253", "v254", "v255", "s90", "s91", "s92", "s93", "s94", "s95"
#define FILL4(a, b, c, d, v) "v_mov_b32 " a ", " v "\n\tv_mov_b32 " b ", " v "\n\tv_mov_b32 " c ", " v "\n\tv_mov_b32 " d ", " v "\n\t"
#define RING5(PRE, O0, O1, O2, O3, O4, MM0, MM1, MM2, MM3, MM4)                                                                        \
  PRE "s_mov_b32 s98, 0x20000\n\t" ARM5 ".Lissue%=:\n\t" RLD(RB0, "s[80:81]", O0) RLD(RB1, "s[82:83]", O1) RLD(RB2, "s[84:85]", O2)      \
  RLD(RB3, "s[86:87]", O3) RLD(RB4, "s[88:89]", O4) "s_mov_b64 exec, -1\n.Lloop%=:\n\t"                                                  \
  RSTEP("4", "v224", RB0, "s[80:81]", O0, MM0) RSTEP("4", "v228", RB1, "s[82:83]", O1, MM1) RSTEP("4", "v232", RB2, "s[84:85]", O2, MM2) \
  RSTEP("4", "v236", RB3, "s[86:87]", O3, MM3) RSTEP("4", "v240", RB4, "s[88:89]", O4, MM4) OR5                                         \
  RTAIL("v224", RB0, "s[80:81]", O0, EXACT5, ARM5)

DI u32 poll_dword(const char* base, u32 off) {
  u32 v;
#pragma unroll 1
  for (int it = 0; it < SPIN_CAP; it++) {
    asm volatile("global_load_dword %0, %1, %2 sc1\n\ts_waitcnt vmcnt(0)" : "=&v"(v) : "v"(off), "s"(base) : "memory");
    if (!__any(v == SENT)) break;
  }
  return v;
}
__global__ void __launch_bounds__(256, 1)
mega(const char* x, const char* kc, const char* vc, const char* wq, const char* wo, const char* w1, const char* b1,
     const char* w2, const char* b2, char* out, char* ws_raw) {
  extern __shared__ __attribute__((aligned(16))) char smem[];
  STAMP(0);
  const u32 t_start = (u32)__builtin_amdgcn_s_memrealtime();
#define HOLD(k) do { if (WAIT_AT == (k)) { while ((u32)__builtin_amdgcn_s_memrealtime() - t_start < WAIT_US * 100u) __builtin_amdgcn_s_sleep(1); } } while (0)
  char* ws = (char*)(((size_t)ws_raw + 0x1FFFFF) & ~(size_t)0x1FFFFF);
  int w_ = __builtin_amdgcn_readfirstlane(threadIdx.x >> 6), wg_ = blockIdx.x;
#define ROLES()                                                                                                             \
  OPAQUE(w_); OPAQUE(wg_);                                                                                                  \
  const int w = w_, wg = wg_, xg = wg & 7, r = wg >> 3, sub = r & 3, b = xg, h = r, grp = (r >> 2) * 4 + (xg & 3);          \
  const bool isA = xg >= 4, isC = !isA && grp < 20, isE = !isA && grp >= 20 && grp < 30, isD = isA && grp < 27;             \
  const int aidx = (xg & 3) * 32 + r, fidx = grp * 4 + sub, kh = grp >= 10 ? 1 : 0, cs = (isC ? grp - 10 * kh : grp - 20) * 4 + sub; \
  const u32 csoff = (u32)(cs / 10) * CH, csr = cs % 10;                                                                      \
  (void)b; (void)h; (void)isD; (void)isE; (void)aidx; (void)fidx; (void)csoff; (void)csr; (void)w
  ROLES();
  const u32 lds0 = (u32)(size_t)(LAS char*)smem;
  St s;
  s.lbase = lds0 + w * LDS_WAVE;
  const size_t kvoff = (size_t)(b * 32 + h) * 1048576 + 786432 + w * 4096;
  s.kb = kc + kvoff;
  s.vb = vc + kvoff;
  u32 tag, aux;
  v4u B[5], W[16];
  {
    LANES();
    const u32 koff = (n >> 2) * 256 + g * 64 + (n & 3) * 16;
    HOLD(0);
    issue_k<3>(s, koff);
    issue_k<6>(s, koff);
    s.tnext = (u32)__builtin_amdgcn_s_memrealtime() + PACE_OFF;
    GAP();
    u32 pf = 0;
    if (w == 2) {
      u64 pc; u32 len;
      asm volatile("s_getpc_b64 %0\n.Lpc%=:\n\ts_mov_b32 %1, .Lmega_end-.Lpc%=" : "=s"(pc), "=s"(len));
      const u32 po = min((u32)(r * 8 + (lane & 7)) * 128u, len - 4u) & ~3u;
      asm volatile("global_load_dword %0, %1, %2" : "=v"(pf) : "v"(po), "s"(pc) : "memory");
    }
    if (w == 1) {
      const u32 po = (lane & (VTOUCH - 1)) * (262144 / VTOUCH);
      asm volatile("global_load_dword %0, %1, %2 offset:-4096 nt" : "=v"(pf) : "v"(po), "s"(s.vb) : "memory");
    }
    u32 epoch;
    {
      const u32 z = 0;
      asm volatile("global_load_dword %0, %1, %2 sc1" : "=v"(epoch) : "v"(z), "s"(ws) : "memory");
      const char* ap = isD ? b1 + fidx * 32 : isE ? b2 + cs * 32 : isC ? x + cs * 32 : x;
      const u32 ao = ecol * 2 + (isC ? eb * 1280 : 0);
      asm volatile("global_load_dword %0, %1, %2" : "=v"(aux) : "v"(ao), "s"(ap) : "memory");
    }
    const u32 own = WS_O + (h >> 2) * CH + (h & 3) * 2048 + b * 128;
    if (w == 3) {
      const u32 rblk = isC ? WS_XP + csoff + csr * 512 + kh * 256 : isD ? WS_H + (fidx / 28) * CH + (fidx % 28) * 256 : isE ? WS_X2 + csoff + csr * 256 : WS_DUMMY;
      const u32 rl = isC ? XP_L : isD ? H_L : 0;
      const u32 ro = g < 2 ? own + g * O_L + (n >> 3) * 1024 + (n & 7) * 16 : rblk + (g - 2) * rl + n * 16;
      const v4u sv = {SENT, SENT, SENT, SENT};
      asm volatile("global_store_dwordx4 %0, %1, %2 sc1\n\tglobal_store_dwordx4 %0, %1, %3 sc1\n\ts_nop 1" ::"v"(ro), "v"(sv), "s"(ws), "s"(ws + SIDE) : "memory");
      if (isE) st_sc1(out, eb * 1280 + cs * 32 + ecol * 2, 0u);
    }
#pragma unroll
    for (int i = 0; i < 5; i++) B[i] = v4u{ONES, ONES, ONES, ONES};
    if (isA) {
      const u32 xo = (n & 7) * 1280 + g * 16;
      const char* xb = x + w * 320;
      u64 sv; const u64 mk = 0x00FF00FF00FF00FFull;
      asm volatile("s_mov_b64 %5, exec\n\ts_mov_b64 exec, %8\n\t"
                   "global_load_dwordx4 %0, %6, %7\n\tglobal_load_dwordx4 %1, %6, %7 offset:64\n\tglobal_load_dwordx4 %2, %6, %7 offset:128\n\t"
                   "global_load_dwordx4 %3, %6, %7 offset:192\n\tglobal_load_dwordx4 %4, %6, %7 offset:256\n\ts_mov_b64 exec, %5"
                   : "+v"(B[0]), "+v"(B[1]), "+v"(B[2]), "+v"(B[3]), "+v"(B[4]), "=&s"(sv) : "v"(xo), "s"(xb), "s"(mk) : "memory");
    }
    const char* wb = isA ? wq + (size_t)(w * 160) * 8192 + aidx * 64 : isC ? wo + (size_t)(kh * 2048 + w * 512) * 1280 + cs * 32 : w2 + (size_t)(w * 448) * 1280 + cs * 32;
    u32 wo_ = isA ? (lane >> 2) * 8192 + (lane & 3) * 16 : (lane >> 1) * 1280 + (lane & 1) * 16;
    int wstep = isA ? 16 * 8192 : 32 * 1280, wn = isA ? 10 : isC ? 16 : isE ? (w == 3 ? 12 : 14) : 0, wrot = isA ? (r & 1) : sub;
    int wpos = 0, wmask = isA ? 1 : 3, wper = isA ? 3 : 2;
    u32 wl = s.lbase + LDS_WST;
    auto w_at = [&](int pos) {
      if (isA && (pos == 6 || pos == 7)) return;
      const int tgt = min(wpos + wper, wn);
#pragma unroll 1
      for (; wpos < tgt; wpos++) {
        int ii = (wpos & ~wmask) | ((wpos + wrot) & wmask);
        if ((wpos | wmask) >= wn) ii = wpos;
        const char* p = wb + ii * wstep;
        const u32 m0v = wl + ii * 1024;
        asm volatile("s_mov_b32 m0, %2\n\ts_nop 0\n\tglobal_load_lds_dwordx4 %0, %1 " WPOL ::"v"(wo_), "s"(p), "s"(m0v) : "memory");
      }
    };
    u32 p1 = 0, p2 = 0;
    if (w == 3) asm volatile("s_load_dword %0, %1, 0x40" : "=s"(p1) : "s"(ws) : "memory");
    w_at(0);
    issue_k<7>(s, koff); w_at(1);
    issue_k<10>(s, koff); w_at(2);
    issue_k<11>(s, koff);
    asm volatile("s_waitcnt vmcnt(12) lgkmcnt(0)" : "+v"(epoch), "+v"(aux), "+v"(pf), "+s"(p1)::"memory");
    STAMP(24);
    HOLD(1);
    tag = (__builtin_amdgcn_readfirstlane(epoch) + 2u) & ~1u;
    if (w == 3) {
      const u32 o1 = own + (r & 1) * SIDE, o2 = own + SIDE - (r & 1) * SIDE;
      u64 t0, t1, t2;
      asm volatile("s_memtime %0\n\ts_load_dword %3, %4, %5\n\ts_waitcnt lgkmcnt(0)\n\ts_memtime %1\n\ts_load_dword %3, %4, %6\n\ts_waitcnt lgkmcnt(0)\n\t"
                   "s_memtime %2\n\ts_waitcnt lgkmcnt(0)"
                   : "=&s"(t0), "=&s"(t1), "=&s"(t2), "=&s"(p2) : "s"(ws), "s"(o1), "s"(o2) : "memory");
      const u32 d1 = (u32)t1 - (u32)t0, d2 = (u32)t2 - (u32)t1;
      const u32 vote = ((d1 < d2 ? (r & 1) : 1 - (r & 1)) ^ (xg >> 2)) & 1;
      if (lane == 0) st_sc1(ws, WS_FLAGS + wg * 4, tag | vote);
    }
    w_at(3);
    if (isA) {
      wb = w1 + (size_t)(w * 160) * 3456 + fidx * 32; wo_ = (lane >> 1) * 3456 + (lane & 1) * 16; wstep = 32 * 3456;
      wn = isD ? 5 : 0; wrot = sub; wl += 10240;
      wpos = 0; wmask = 3; wper = 2;
    }
    issue_k<14>(s, koff); w_at(6);
    issue_k<15>(s, koff); w_at(7);
    STAMP(25);
    const int q4 = (lane >> 2) & 3, p4 = lane & 3;
#pragma unroll
    for (int i = 0; i < 16; i++) asm volatile("" : "=v"(W[i]));
    if (isA) {
      asm volatile("s_waitcnt vmcnt(8)" : "+v"(B[0]), "+v"(B[1]), "+v"(B[2]), "+v"(B[3]), "+v"(B[4])::"memory");
      const u32 a0 = s.lbase + LDS_WST + (8 * g + q4) * 64 + 8 * p4;
#pragma unroll
      for (int i = 0; i < 10; i++) W[i] = tr_frag(a0 + (i >> 1) * 2048 + (i & 1) * 32, 256);
    }
    STAMP(1);
    float res_lo = bf_lo(aux), res_hi = bf_hi(aux);
    u32 side = 0;

#pragma unroll 1
    for (int l = 0; l < 2; l++) {
      const bool first = l == 0;
      OPAQUE(ws);
      ROLES();
      if (isA) {
        LANES();
        u32 wa = n < 8 ? lds0 + SC_ROLE + w * 1024 + (g >> 1) * 256 + n * 32 + (g & 1) * 16 : lds0 + SC_CS + w * 128 + g * 16;
        u32 sa = lds0 + SC_STAT + w * 64 + n * 8 + (g == 2 ? 4 : 0);
        u32 qo = (eb * 2 + (aidx >> 6)) * CH + (((eb >> 2) * SIDE) ^ side) + (aidx & 63) * 128 + (8 * w + 4 * eh + ec) * 8;
        PIN(wa); PIN(sa); PIN(qo);
        f32x4 t0, t1, gr;
        if (first) {
          asm volatile("s_nop 1\n\t" MF "%0, %3, %13, 0\n\t" MF "%1, %4, %13, 0\n\t" MF "%2, %13, %13, 0\n\t"
                       MF "%0, %5, %14, %0\n\t" MF "%1, %6, %14, %1\n\t" MF "%2, %14, %14, %2\n\t"
                       MF "%0, %7, %15, %0\n\t" MF "%1, %8, %15, %1\n\t" MF "%2, %15, %15, %2\n\t"
                       MF "%0, %9, %16, %0\n\t" MF "%1, %10, %16, %1\n\t" MF "%2, %16, %16, %2\n\t"
                       MF "%2, %17, %17, %2\n\t" MF "%0, %11, %17, %0\n\t" MF "%1, %12, %17, %1\n\ts_nop 7"
                       : "=&{v[216:219]}"(t0), "=&v"(t1), "=&v"(gr)
                       : "v"(W[0]), "v"(W[1]), "v"(W[2]), "v"(W[3]), "v"(W[4]), "v"(W[5]), "v"(W[6]), "v"(W[7]), "v"(W[8]), "v"(W[9]),
                         "v"(B[0]), "v"(B[1]), "v"(B[2]), "v"(B[3]), "v"(B[4]));
        } else {
          const u32 xo = g * 128 + (n & 7) * 16;
          const char* xb = ws + WS_X2 + w * CH + (SIDE ^ side);
          const u64 mk = 0x00FF00FF00FF00FFull;
#define AMM(B, WA, WB, C0, C1, C2) MF "%0, " WA ", " B ", " C0 "\n\t" MF "%2, " B ", " B ", " C2 "\n\t" MF "%1, " WB ", " B ", " C1 "\n\t"
          asm volatile(RING5("v_mov_b32 v223, 0x3f803f80\n\t" FILL4("v224", "v225", "v226", "v227", "v223") FILL4("v228", "v229", "v230", "v231", "v223")
                             FILL4("v232", "v233", "v234", "v235", "v223") FILL4("v236", "v237", "v238", "v239", "v223") FILL4("v240", "v241", "v242", "v243", "v223"),
                             "0", "512", "1024", "1536", "2048",
                             AMM(RB0, "%3", "%4", "0", "0", "0"), AMM(RB1, "%5", "%6", "%0", "%1", "%2"), AMM(RB2, "%7", "%8", "%0", "%1", "%2"),
                             AMM(RB3, "%9", "%10", "%0", "%1", "%2"), AMM(RB4, "%11", "%12", "%0", "%1", "%2"))
                       : "=&{v[216:219]}"(t0), "=&v"(t1), "=&v"(gr)
                       : "v"(W[0]), "v"(W[1]), "v"(W[2]), "v"(W[3]), "v"(W[4]), "v"(W[5]), "v"(W[6]), "v"(W[7]), "v"(W[8]), "v"(W[9]),
                         [go] "v"(xo), [gb] "s"(xb), [full] "s"(mk)
                       : CLOB5);
          STAMP(9);
        }
        if (n <= 8) {
          lds_st<f32x4>(wa, t0);
          lds_st<f32x4>(wa + (n < 8 ? 512 : 64), t1);
        }
        if (n < 8 && (g == 2 || g == (n >> 2))) {
          const float gv = g == 2 ? gr[0] : (n & 3) == 0 ? gr[0] : (n & 3) == 1 ? gr[1] : (n & 3) == 2 ? gr[2] : gr[3];
          lds_st<float>(sa, gv);
        }
        lds_barrier();
        if (w < 2) {
          f32x2 acc = {0.f, 0.f}, csum = {0.f, 0.f}, st = {0.f, 0.f};
#pragma unroll
          for (int ww = 0; ww < 4; ww++) {
            st += lds_ld<f32x2>(lds0 + SC_STAT + ww * 64 + eb * 8);
            acc += lds_ld<f32x2>(lds0 + SC_ROLE + ww * 1024 + w * 512 + lane * 8);
            csum += lds_ld<f32x2>(lds0 + SC_CS + ww * 128 + w * 64 + ecol * 4);
          }
          const float mean = st[1] * (1.f / 640.f), var = fmaxf(st[0] * (1.f / 640.f) - mean * mean, 0.f);
          const float rs = __builtin_amdgcn_rsqf(var + 1e-5f);
          const v2u gq = {pk_bf16(rs * (acc[0] - mean * csum[0]), rs * (acc[1] - mean * csum[1])), tag};
          st2_sc1(ws + WS_Q + l * Q_L, qo, gq);
        }
        if (!first) STAMP(10);
      }
      LANES();
      OPAQUE(s.lbase);
      const u32 qoff = (4 * g + (n >> 2)) * 32;
      const char* qb = ws + WS_Q + l * Q_L + (b * 2 + (h >> 4)) * CH + (((b >> 2) * SIDE) ^ side) + (h & 15) * 512;
      u32 la = s.lbase + lane * 16, vrun = lane * 16, vhi = 16;
      v4u qg0, qg1, ring[6];
      bool ok = false;
      if (first) {
        const u32 koff2 = (n >> 2) * 256 + g * 64 + (n & 3) * 16;
        OPAQUE(s.kb); OPAQUE(s.vb);
        STAMP(26);
        issue_k<0>(s, koff2); w_at(8);
        issue_k<1>(s, koff2); w_at(9);
        issue_k<2>(s, koff2); w_at(10);
        issue_k<4>(s, koff2);
        issue_k<5>(s, koff2);
        STAMP(27);
        VMWAIT(8);
        STAMP(28);
        const int q4 = (lane >> 2) & 3, p4 = lane & 3;
        if (isA) {
          const u32 d0 = s.lbase + LDS_WST + 10240 + (8 * g + q4) * 32 + 8 * p4;
#pragma unroll
          for (int i = 0; i < 5; i++) W[10 + i] = tr_frag(d0 + i * 1024, 128);
          W[15] = v4u{ONES, ONES, ONES, ONES};
          f32x4 c1;
          asm volatile("s_nop 1\n\t" MF "%0, %1, %6, 0\n\t" MF "%0, %2, %6, %0\n\t" MF "%0, %3, %6, %0\n\t" MF "%0, %4, %6, %0\n\t" MF "%0, %5, %6, %0\n\ts_nop 7"
                       : "=&v"(c1) : "v"(W[10]), "v"(W[11]), "v"(W[12]), "v"(W[13]), "v"(W[14]), "v"(W[15]));
          if (n == 0) lds_st<f32x4>(lds0 + SC_CSD + w * 64 + g * 16, c1);
        } else {
          const u32 c0 = s.lbase + LDS_WST + (32 * (p4 >> 1) + 8 * g + q4) * 32 + 8 * (p4 & 1);
#pragma unroll
          for (int i = 0; i < 16; i++) W[i] = tr_frag(c0 + (i >> 1) * 2048 + (i & 1) * 16, 128);
          if (!isC) { W[14] = v4u{0u, 0u, 0u, 0u}; W[15] = W[14]; if (w == 3) { W[12] = W[14]; W[13] = W[14]; } }
        }
        asm volatile("s_waitcnt lgkmcnt(0)" ::: "memory");
        issue_k<8>(s, koff2); issue_k<9>(s, koff2); issue_k<12>(s, koff2); issue_k<13>(s, koff2);
        asm volatile("global_load_dwordx4 %0, %2, %3 sc1\n\tglobal_load_dwordx4 %1, %2, %3 offset:16 sc1"
                     : "=&v"(qg0), "=&v"(qg1) : "v"(qoff), "s"(qb) : "memory");
        STAMP(29);
        HOLD(2);
        u32 lo = 0;
        vhi = 2 + 2 * w;
#pragma unroll 1
        for (;;) {
          issue_v_range<0>(s, vrun, lo, vhi);
          asm volatile("s_waitcnt vmcnt(8)" : "+v"(qg0), "+v"(qg1)::"memory");
          ok = __all(qg0[1] == tag && qg0[3] == tag && qg1[1] == tag && qg1[3] == tag);
          if (ok || vhi == 16) break;
          lo = vhi; vhi = 16;
        }
      } else asm volatile("" : "=v"(qg0), "=v"(qg1));
      asm volatile("ds_read_b128 %0, %6\n\tds_read_b128 %1, %6 offset:1024\n\tds_read_b128 %2, %6 offset:4096\n\tds_read_b128 %3, %6 offset:5120\n\t"
                   "ds_read_b128 %4, %6 offset:8192\n\tds_read_b128 %5, %6 offset:9216\n\ts_waitcnt lgkmcnt(0)"
                   : "=&v"(ring[0]), "=&v"(ring[1]), "=&v"(ring[2]), "=&v"(ring[3]), "=&v"(ring[4]), "=&v"(ring[5]) : "v"(la) : "memory");
      Pv p;
#pragma unroll
      for (int rr = 0; rr < 4; rr++) {
        const u32 src = n < 8 ? 0x0100u : 0x0302u;
        p.sel[rr] = ((n & 7) >> 1) != rr ? 0x0c0c0c0cu : (n & 1) ? (src << 16) | 0x0c0cu : 0x0c0c0000u | src;
        PIN(p.sel[rr]);
      }
      float res[4] = {0.f, 0.f, 0.f, 0.f};
      PIN(res[0]); PIN(res[1]); PIN(res[2]); PIN(res[3]);
#pragma unroll 1
      for (int it = 0; !ok && it < SPIN_CAP; it++) {
        asm volatile("global_load_dwordx4 %0, %2, %3 sc1\n\tglobal_load_dwordx4 %1, %2, %3 offset:16 sc1\n\ts_waitcnt vmcnt(0)"
                     : "=&v"(qg0), "=&v"(qg1) : "v"(qoff), "s"(qb) : "memory");
        ok = __all(qg0[1] == tag && qg0[3] == tag && qg1[1] == tag && qg1[3] == tag);
      }
      if (!first) HOLD(4);
      if (first) STAMP(2); else STAMP(11);
      CSTAMP(16);
      {
        v4u bq[4];
#pragma unroll
        for (int u = 0; u < 4; u++) { const bool on = (n & 3) == u; bq[u] = v4u{on ? qg0[0] : 0u, on ? qg0[2] : 0u, on ? qg1[0] : 0u, on ? qg1[2] : 0u}; }
        asm volatile("s_nop 1" : "+v"(bq[0]), "+v"(bq[1]), "+v"(bq[2]), "+v"(bq[3]));
        f32x4 d[2][2];
        qk_period<0>(s, bq, d, res, ring, la); qk_period<1>(s, bq, d, res, ring, la); qk_period<2>(s, bq, d, res, ring, la);
        qk_period<3>(s, bq, d, res, ring, la); qk_period<4>(s, bq, d, res, ring, la); qk_period<5>(s, bq, d, res, ring, la);
        qk_period<6>(s, bq, d, res, ring, la); qk_period<7>(s, bq, d, res, ring, la); qk_period<8>(s, bq, d, res, ring, la);
      }
      CSTAMP(17);
      constexpr float C2 = 0.08838834764831845f * 1.4426950408889634f;
      const float mw = wave_max4(res[0], res[1], res[2], res[3]) * C2;
      float pr[4];
#pragma unroll
      for (int z = 0; z < 4; z++) pr[z] = __builtin_amdgcn_exp2f(__builtin_fmaf(res[z], C2, -mw));
      p.X[0] = pk_bf16(pr[0], pr[1]); p.X[1] = pk_bf16(pr[2], pr[3]);
      float lsum = (pr[0] + pr[1]) + (pr[2] + pr[3]);
      if (first) {
        v4u fl;
        const char* fb = ws + WS_FLAGS;
        const u32 fo = lane * 16;
        asm volatile("global_load_dwordx4 %0, %1, %2 sc1" : "=&v"(fl) : "v"(fo), "s"(fb) : "memory");
        issue_v_range<2>(s, vrun, vhi, 16);
        STAMP(30);
        if (vhi == 16) VMWAIT(0);
        asm volatile("s_waitcnt vmcnt(16)" : "+v"(fl)::"memory");
#pragma unroll 1
        for (int it = 0; it < SPIN_CAP; it++) {
          if (__all(((fl[0] ^ tag) | (fl[1] ^ tag) | (fl[2] ^ tag) | (fl[3] ^ tag)) < 2u)) break;
          asm volatile("global_load_dwordx4 %0, %1, %2 sc1\n\ts_waitcnt vmcnt(0)" : "=&v"(fl) : "v"(fo), "s"(fb) : "memory");
        }
        const int votes = __popcll(__ballot(fl[0] & 1)) + __popcll(__ballot(fl[1] & 1)) + __popcll(__ballot(fl[2] & 1)) + __popcll(__ballot(fl[3] & 1));
        side = votes > 128 ? SIDE : 0;
      }
      p.d1 = f32x4{0.f, 0.f, 0.f, 0.f}; p.d2 = p.d1;
      p.xb = bcast<0>(p.X[0]);
#pragma unroll
      for (int rr = 0; rr < 4; rr++) p.a[0][rr] = __builtin_amdgcn_perm(p.xb, p.xb, p.sel[rr]);
      p.xb = bcast<0>(p.X[1]);
      p.a[1][0] = __builtin_amdgcn_perm(p.xb, p.xb, p.sel[0]);
      asm volatile("s_nop 1" : "+v"(p.a[0]), "+v"(p.d1), "+v"(p.d2));
      GAP();
      CSTAMP(18);
      pv_step<0>(s, p); pv_step<1>(s, p); pv_step<2>(s, p); pv_step<3>(s, p); pv_step<4>(s, p); pv_step<5>(s, p); pv_step<6>(s, p); pv_step<7>(s, p);
      pv_step<8>(s, p); pv_step<9>(s, p); pv_step<10>(s, p); pv_step<11>(s, p); pv_step<12>(s, p); pv_step<13>(s, p); pv_step<14>(s, p); pv_step<15>(s, p);
      {
        PIN(lsum);
        const float lw = wave_sum(lsum);
        LANES();
        ROLES();
        GAP();
        asm volatile("" : "+v"(p.d1), "+v"(p.d2));
        CSTAMP(19);
        if (first) STAMP(3); else STAMP(12);
        lds_st<f32x4>(lds0 + SC_ATT + (w * 2 + (g >> 1)) * 512 + n * 32 + (g & 1) * 16, g < 2 ? p.d1 : p.d2);
        if (lane == 0) lds_st<f32x2>(lds0 + SC_ML + w * 8, f32x2{mw, lw});
        lds_barrier();
        if (w == 0) {
          const f32x4 ma = lds_ld<f32x4>(lds0 + SC_ML), mb = lds_ld<f32x4>(lds0 + SC_ML + 16);
          f32x2 part[4];
#pragma unroll
          for (int ww = 0; ww < 4; ww++) part[ww] = lds_ld<f32x2>(lds0 + SC_ATT + ww * 1024 + lane * 8) + lds_ld<f32x2>(lds0 + SC_ATT + ww * 1024 + 512 + lane * 8);
          const float M = fmaxf(fmaxf(ma[0], ma[2]), fmaxf(mb[0], mb[2]));
          const float f0 = __builtin_amdgcn_exp2f(ma[0] - M), f1 = __builtin_amdgcn_exp2f(ma[2] - M), f2 = __builtin_amdgcn_exp2f(mb[0] - M), f3 = __builtin_amdgcn_exp2f(mb[2] - M);
          const float inv = __builtin_amdgcn_rcpf((f0 * ma[1] + f1 * ma[3]) + (f2 * mb[1] + f3 * mb[3]));
          if (first) HOLD(3); else HOLD(5);
          const f32x2 o = (f0 * part[0] + f1 * part[1]) + (f2 * part[2] + f3 * part[3]);
          st_sc1(ws + WS_O + l * O_L + (h >> 2) * CH + side + (h & 3) * 2048 + b * 128, eh * 1024 + (lane & 31) * 4, min(pk_bf16(o[0] * inv, o[1] * inv), SENT - 1));
        }
        CSTAMP(20);
      }
      if (first) STAMP(4); else STAMP(13);
      {
      ROLES();
      if (isC || isE || isD) {
        LANES();
        OPAQUE(ws);
        char* pub = isD ? ws + WS_H + l * H_L + (fidx / 28) * CH + side + (fidx % 28) * 256
                        : isC ? ws + WS_XP + l * XP_L + csoff + (SIDE ^ side) + csr * 512 + kh * 256
                              : first ? ws + WS_X2 + csoff + (SIDE ^ side) + csr * 256 : out + cs * 32;
        u32 po = isE && !first ? eb * 1280 + ecol * 2 : lane * 4;
        OPAQUE(pub); PIN(po);
        float xr_lo = 0.f, xr_hi = 0.f;
        if (isD) {
          f32x4 t0, t1, t2;
          const char* xb = ws + WS_XP + l * XP_L + w * CH + (SIDE ^ side) + 2048;
          const u32 xo = (g >> 1) * 512 + (n >> 3) * 256 + (g & 1) * 128 + (n & 7) * 16;
          const bool isG = (g & 1) == ((n >> 2) & 1);
          u32 wa = lds0 + SC_ROLE + (w * 2 + (n >> 3)) * 512 + (g >> 1) * 256 + (n & 7) * 32 + (g & 1) * 16;
          u32 sa = lds0 + SC_STAT + w * 256 + (n & 7) * 32 + (isG ? 0 : 16) + (g >> 1) * 8 + (n >> 3) * 4;
          PIN(wa); PIN(sa);
          const u64 mk = ~0ull;
#define DMM(B, WA, C0, C1, C2) MF "%0, " WA ", " B ", " C0 "\n\t" MF "%1, " B ", " B ", " C1 "\n\t" MF "%2, %8, " B ", " C2 "\n\t"
          asm volatile(RING5("", "-2048", "-1024", "0", "1024", "2048", DMM(RB0, "%3", "0", "0", "0"), DMM(RB1, "%4", "%0", "%1", "%2"),
                             DMM(RB2, "%5", "%0", "%1", "%2"), DMM(RB3, "%6", "%0", "%1", "%2"), DMM(RB4, "%7", "%0", "%1", "%2"))
                       : "=&{v[216:219]}"(t0), "=&v"(t1), "=&v"(t2)
                       : "v"(W[10]), "v"(W[11]), "v"(W[12]), "v"(W[13]), "v"(W[14]), "v"(W[15]), [go] "v"(xo), [gb] "s"(xb), [full] "s"(mk)
                       : CLOB5);
          if (first) STAMP(5); else STAMP(14);
          lds_st<f32x4>(wa, t0);
          const float gv = !isG ? t2[0] : (n & 3) == 0 ? t1[0] : (n & 3) == 1 ? t1[1] : (n & 3) == 2 ? t1[2] : t1[3];
          lds_st<float>(sa, gv);
        } else {
          const char* gb = ws + (isC ? WS_O + l * O_L + (kh * 4 + w) * CH : WS_H + l * H_L + w * CH) + side + 4096;
          if (isE) {
            if (w == 0) {
              const char* xb = ws + WS_XP + l * XP_L + csoff + (SIDE ^ side) + csr * 512;
              const u32 p1 = poll_dword(xb, lane * 4), p2 = poll_dword(xb + 256, lane * 4);
              xr_lo = bf_lo(p1) + bf_lo(p2) + res_lo; xr_hi = bf_hi(p1) + bf_hi(p2) + res_hi;
            }
          }
          f32x4 t0, t1;
          const u32 go = isC ? (n & 7) * 128 + (n >> 3) * 64 + g * 16 : (n >> 3) * 512 + g * 128 + (n & 7) * 16;
          u32 wa = lds0 + SC_ROLE + (w * 2 + (n >> 3)) * 512 + (n & 7) * 32 + (g & 1) * 16;
          PIN(wa);
          const u64 mk = ~0ull;
          const int nf = __builtin_amdgcn_readfirstlane(isC ? 8 : w != 3 ? 7 : 6);
#define CMM(B, WA, WB, C0, C1) MF "%0, " WA ", " B ", " C0 "\n\t" MF "%1, " WB ", " B ", " C1 "\n\t"
          asm volatile(FILL4("v248", "v249", "v250", "v251", "0") FILL4("v252", "v253", "v254", "v255", "0") "s_mov_b32 s98, 0x20000\n\t" ARM8 ".Lissue%=:\n\t"
                       RLD(RB7, "s[94:95]", "3072") RLD(RB6, "s[92:93]", "2048") RLD(RB0, "s[80:81]", "-4096") RLD(RB1, "s[82:83]", "-3072")
                       RLD(RB2, "s[84:85]", "-2048") RLD(RB3, "s[86:87]", "-1024") RLD(RB4, "s[88:89]", "0") RLD(RB5, "s[90:91]", "1024")
                       "s_mov_b64 exec, -1\n.Lloop%=:\n\t"
                       RSTEP("7", "v252", RB7, "s[94:95]", "3072", CMM(RB7, "%16", "%17", "0", "0")) RSTEP("7", "v248", RB6, "s[92:93]", "2048", CMM(RB6, "%14", "%15", "%0", "%1"))
                       RSTEP("7", "v224", RB0, "s[80:81]", "-4096", CMM(RB0, "%2", "%3", "%0", "%1")) RSTEP("7", "v228", RB1, "s[82:83]", "-3072", CMM(RB1, "%4", "%5", "%0", "%1"))
                       RSTEP("7", "v232", RB2, "s[84:85]", "-2048", CMM(RB2, "%6", "%7", "%0", "%1")) RSTEP("7", "v236", RB3, "s[86:87]", "-1024", CMM(RB3, "%8", "%9", "%0", "%1"))
                       RSTEP("7", "v240", RB4, "s[88:89]", "0", CMM(RB4, "%10", "%11", "%0", "%1")) OR5
                       "s_or_b64 vcc, s[92:93], s[94:95]\n\ts_or_b64 s[96:97], s[96:97], vcc\n\t"
                       RSTEP("7", "v244", RB5, "s[90:91]", "1024", CMM(RB5, "%12", "%13", "%0", "%1"))
                       "s_or_b64 s[96:97], s[96:97], s[90:91]\n\ts_nop 1\n\t"
                       RTAIL("v224", RB0, "s[80:81]", "-4096", EXACT8, ARM8)
                       : "=&{v[216:219]}"(t0), "=&v"(t1)
                       : "v"(W[0]), "v"(W[1]), "v"(W[2]), "v"(W[3]), "v"(W[4]), "v"(W[5]), "v"(W[6]), "v"(W[7]), "v"(W[8]), "v"(W[9]), "v"(W[10]), "v"(W[11]),
                         "v"(W[12]), "v"(W[13]), "v"(W[14]), "v"(W[15]), [go] "v"(go), [gb] "s"(gb), [full] "s"(mk), [nf] "s"(nf)
                       : CLOB8);
          if (first) STAMP(5); else STAMP(14);
          if ((g >> 1) == (n >> 3)) {
            lds_st<f32x4>(wa, t0);
            lds_st<f32x4>(wa + 256, t1);
          }
        }
        lds_barrier();
        if (w == 0) {
          f32x2 pa[8];
#pragma unroll
          for (int i = 0; i < 8; i++) pa[i] = lds_ld<f32x2>(lds0 + SC_ROLE + i * 512 + lane * 8);
          if (isD) {
            f32x4 gq = {0.f, 0.f, 0.f, 0.f}; f32x2 rq = {0.f, 0.f}, csD = {0.f, 0.f};
#pragma unroll
            for (int ww = 0; ww < 4; ww++) {
              gq += lds_ld<f32x4>(lds0 + SC_STAT + ww * 256 + eb * 32);
              rq += lds_ld<f32x2>(lds0 + SC_STAT + ww * 256 + eb * 32 + 16);
              csD += lds_ld<f32x2>(lds0 + SC_CSD + ww * 64 + ecol * 4);
            }
            const f32x2 acc = ((pa[0] + pa[1]) + (pa[2] + pa[3])) + ((pa[4] + pa[5]) + (pa[6] + pa[7]));
            const float mean = (rq[0] + rq[1]) * (1.f / 640.f), var = fmaxf(((gq[0] + gq[1]) + (gq[2] + gq[3])) * (1.f / 640.f) - mean * mean, 0.f);
            const float rs = __builtin_amdgcn_rsqf(var + 1e-5f * (var + 1e-5f));
            const float y0 = rs * (acc[0] - mean * csD[0]) + res_lo, y1 = rs * (acc[1] - mean * csD[1]) + res_hi;
            constexpr float GC = -2.f * 0.7978845608028654f * 1.4426950408889634f;
            const float e0 = __builtin_amdgcn_exp2f(y0 * (GC + GC * 0.044715f * y0 * y0)), e1 = __builtin_amdgcn_exp2f(y1 * (GC + GC * 0.044715f * y1 * y1));
            const u32 hv = pk_bf16(y0 * __builtin_amdgcn_rcpf(1.f + e0), y1 * __builtin_amdgcn_rcpf(1.f + e1));
            st_sc1(pub, po, min(hv, SENT - 1));
            if (first) STAMP(6); else STAMP(15);
          } else {
            const f32x2 acc = ((pa[0] + pa[1]) + (pa[2] + pa[3])) + ((pa[4] + pa[5]) + (pa[6] + pa[7]));
            if (isC) {
              const u32 xv = kh ? pk_bf16(acc[0], acc[1]) : pk_bf16(acc[0] + res_lo, acc[1] + res_hi);
              st_sc1(pub, po, min(xv, SENT - 1));
              if (first) STAMP(6); else STAMP(15);
              if (first && kh == 0) {
                const u32 p1 = poll_dword(ws + WS_X2 + csoff + (SIDE ^ side) + csr * 256, lane * 4);
                res_lo = bf_lo(p1); res_hi = bf_hi(p1);
              }
            } else {
              const u32 xv = pk_bf16(xr_lo + acc[0], xr_hi + acc[1]);
              st_sc1(pub, po, min(xv, SENT - 1));
              if (!first && cs == 0 && lane == 0) st_sc1(ws, WS_EPOCH, tag);
              if (first) STAMP(6); else STAMP(15);
            }
          }
        }
      }
      }
      asm volatile("" : "=v"(B[0]), "=v"(B[1]), "=v"(B[2]), "=v"(B[3]), "=v"(B[4]), "=v"(wo_));
      asm volatile("" : "=s"(wb), "=s"(wstep), "=s"(wn), "=s"(wpos), "=s"(wrot), "=s"(wl), "=s"(s.kb), "=s"(s.vb), "=s"(s.tnext));
      asm volatile("" : "=s"(wmask), "=s"(wper));
    }
  }
  asm volatile(".Lmega_end:");
}

extern "C" void kernel_launch(void* const* d_in, const int* in_sizes, int n_in, void* d_out, int out_size, void* d_ws,
                              size_t ws_size, hipStream_t stream) {
  (void)hipFuncSetAttribute((const void*)mega, hipFuncAttributeMaxDynamicSharedMemorySize, LDS_BYTES);
  mega<<<256, 256, LDS_BYTES, stream>>>((const char*)d_in[0], (const char*)d_in[1], (const char*)d_in[2], (const char*)d_in[3],
                                        (const char*)d_in[4], (const char*)d_in[6], (const char*)d_in[7], (const char*)d_in[8],
                                        (const char*)d_in[9], (char*)d_out, (char*)d_ws);
}
```

```cpp
#include <hip/hip_runtime.h>
#include <stdint.h>

typedef unsigned u32;
typedef unsigned long long u64;
typedef u32 v4u __attribute__((ext_vector_type(4)));
typedef u32 v2u __attribute__((ext_vector_type(2)));
typedef u32 v8u __attribute__((ext_vector_type(8)));
typedef u32 v12u __attribute__((ext_vector_type(12)));
typedef u32 v16u __attribute__((ext_vector_type(16)));
typedef u32 v32u __attribute__((ext_vector_type(32)));
typedef float f32x4 __attribute__((ext_vector_type(4)));
typedef float f32x2 __attribute__((ext_vector_type(2)));
typedef __bf16 bf16x2 __attribute__((ext_vector_type(2)));
#define LAS __attribute__((address_space(3)))
#define DI __device__ __forceinline__
#define COLD(c) __builtin_expect((c), 0)
#define GAP() __builtin_amdgcn_sched_barrier(0)

constexpr int VOTE_R = 28;
constexpr int CD0 = 3, CD1 = 9, QUIET = 7;
constexpr int PACE_OFF = 30, PACE1 = 83, PACE2 = 71, PACE3 = 57;
constexpr u32 CH = 16384, SIDE = 8192;
constexpr u32 WS_EPOCH = 0, WS_CAL = 0x1000, WS_FLAGS = 0x400;
constexpr u32 WS_Q = CH, Q_L = 16 * CH;
constexpr u32 q_off(u32 row, u32 a) { return (row * 2 + (a >> 6)) * CH + (row >> 2) * SIDE + (a & 63) * 128; }
constexpr u32 WS_O = WS_Q + 2 * Q_L, O_L = 8 * CH;
constexpr u32 WS_XP = WS_O + 2 * O_L, XP_L = 4 * CH;
constexpr u32 WS_H = WS_XP + 2 * XP_L, H_L = 4 * CH;
constexpr u32 WS_X2 = WS_H + 2 * H_L;
constexpr int RESET_CHUNKS = (WS_X2 + 4 * CH - WS_Q - Q_L) / CH;
constexpr u32 SC_ROLE = 8192, SC_STAT = 12288, SC_CS = 13312, SC_CSD = 13824, SC_ML = 14080, SC_DUMP = 14336, LDS_K = 16384, LDS_WAVE = 36864,
              LDS_BYTES = 163840;
constexpr u32 SENT = 0xFFFFFFFFu, ONES = 0x3F803F80u;
constexpr int SPIN_CAP = 1 << 17;

DI u32 pk_bf16(float lo, float hi) {
  f32x2 v = {lo, hi};
  return __builtin_bit_cast(u32, __builtin_convertvector(v, bf16x2));
}
DI float bf_lo(u32 x) { return __builtin_bit_cast(float, x << 16); }
DI float bf_hi(u32 x) { return __builtin_bit_cast(float, x & 0xffff0000u); }
DI float wave_max4(float a, float b, float c, float d, u32 l8, f32x4& d1, f32x4& d2, u32& m0, u32& m1, u32& ix) {
  float x, y;
  asm volatile("v_max3_f32 %0, %7, %8, %9\n\tv_max_f32 %0, %0, %10\n\tv_mov_b64 v[208:209], 0\n\tv_mov_b64 v[210:211], 0\n\t"
               "v_max_f32_dpp %0, %0, %0 quad_perm:[1,0,3,2] row_mask:0xf bank_mask:0xf\n\tv_mov_b64 v[212:213], 0\n\tv_mov_b64 v[214:215], 0\n\t"
               "v_max_f32_dpp %0, %0, %0 quad_perm:[2,3,0,1] row_mask:0xf bank_mask:0xf\n\tv_bfe_u32 %6, %11, 3, 2\n\tv_mul_u32_u24 %6, 0x5555, %6\n\t"
               "v_max_f32_dpp %0, %0, %0 row_ror:4 row_mask:0xf bank_mask:0xf\n\tv_bfe_i32 %5, %11, 6, 1\n\tv_xor_b32 %5, 0xffff, %5\n\t"
               "v_max_f32_dpp %0, %0, %0 row_ror:8 row_mask:0xf bank_mask:0xf\n\tv_mov_b32 %1, %0\n\tv_cndmask_b32 %4, %5, 0, %12\n\tv_cndmask_b32 %5, 0, %5, %12\n\t"
               "v_permlane16_swap_b32 %0, %1\n\tv_max_f32 %0, %0, %1\n\tv_mov_b32 %1, %0\n\ts_nop 1\n\t"
               "v_permlane32_swap_b32 %0, %1\n\tv_max_f32 %0, %0, %1"
               : "=&{v219}"(x), "=&{v220}"(y), "=&{v[208:211]}"(d1), "=&{v[212:215]}"(d2), "=&{v216}"(m0), "=&{v217}"(m1), "=&{v218}"(ix)
               : "{v204}"(a), "{v205}"(b), "{v206}"(c), "{v207}"(d), "v"(l8), "s"(0xF0F0F0F0F0F0F0F0ull));
  return x;
}
DI void lds_barrier() { asm volatile("s_waitcnt lgkmcnt(0)\n\ts_barrier" ::: "memory"); }
DI void st_sc1(char* base, u32 off, u32 v) { asm volatile("global_store_dword %0, %1, %2 sc1" ::"v"(off), "v"(v), "s"(base) : "memory"); }
DI void st2_sc1(char* base, u32 off, v2u v) { asm volatile("global_store_dwordx2 %0, %1, %2 sc1" ::"v"(off), "v"(v), "s"(base) : "memory"); }
template <typename T> DI T lds_ld(u32 a) { return *(const LAS T*)(size_t)a; }
template <typename T> DI void lds_st(u32 a, T v) { *(LAS T*)(size_t)a = v; }
DI int lane_id() { int l; asm volatile("v_mbcnt_lo_u32_b32 %0, -1, 0\n\tv_mbcnt_hi_u32_b32 %0, -1, %0" : "=v"(l)); return l; }
#define LANES() const int lane = lane_id(), g = lane >> 4, n = lane & 15, eh = lane >> 5, eb = (lane >> 2) & 7, ec = lane & 3, ecol = 8 * eh + 2 * ec; (void)g; (void)n; (void)eb; (void)ecol
#define OPAQUE(p) asm volatile("" : "+s"(p))
#define PIN(v) asm volatile("" : "+v"(v))
#define MF "v_mfma_f32_16x16x32_bf16 "
#define NAP_IF(c, t) asm volatile("s_cmp_eq_u32 %0, 0\n\ts_cbranch_scc1 .Ln%=\n\ts_sleep %1\n.Ln%=:" ::"s"(__builtin_amdgcn_readfirstlane((int)(c))), "i"(t) : "scc", "memory")
#define VMWAIT(n) asm volatile("s_waitcnt vmcnt(%0)" ::"i"(n) : "memory")

constexpr bool k_in_lds(int c) { return (0x5AB55AB5u >> c) & 1; }
constexpr int k_lidx(int c) { return __builtin_popcount(0x5AB55AB5u & ((1u << c) - 1)); }
constexpr int k_imm(int c) { return ((c >> 3) & 1) * 2048; }
constexpr int KORD[32] = {0, 8, 1, 9, 2, 10, 3, 11, 5, 13, 6, 15, 16, 24, 17, 25, 18, 26, 19, 27, 21, 29, 22, 31, 4, 12, 7, 14, 20, 28, 23, 30};
#define WR0 "v[112:115]"
#define WL0 "v[112:113]"
#define WH0 "v[114:115]"
#define WR1 "v[116:119]"
#define WL1 "v[116:117]"
#define WH1 "v[118:119]"
#define WR2 "v[120:123]"
#define WL2 "v[120:121]"
#define WH2 "v[122:123]"
#define WR3 "v[124:127]"
#define WL3 "v[124:125]"
#define WH3 "v[126:127]"
#define WR4 "v[128:131]"
#define WL4 "v[128:129]"
#define WH4 "v[130:131]"
#define WR5 "v[132:135]"
#define WL5 "v[132:133]"
#define WH5 "v[134:135]"
#define WR6 "v[136:139]"
#define WL6 "v[136:137]"
#define WH6 "v[138:139]"
#define WR7 "v[140:143]"
#define WL7 "v[140:141]"
#define WH7 "v[142:143]"
#define WR8 "v[144:147]"
#define WL8 "v[144:145]"
#define WH8 "v[146:147]"
#define WR9 "v[148:151]"
#define WL9 "v[148:149]"
#define WH9 "v[150:151]"
#define WR10 "v[152:155]"
#define WL10 "v[152:153]"
#define WH10 "v[154:155]"
#define WR11 "v[156:159]"
#define WL11 "v[156:157]"
#define WH11 "v[158:159]"
#define WR12 "v[160:163]"
#define WL12 "v[160:161]"
#define WH12 "v[162:163]"
#define WR13 "v[164:167]"
#define WL13 "v[164:165]"
#define WH13 "v[166:167]"
#define WR14 "v[168:171]"
#define WL14 "v[168:169]"
#define WH14 "v[170:171]"
#define WR15 "v[172:175]"
#define WL15 "v[172:173]"
#define WH15 "v[174:175]"
#define WC(c, i) c "{" WR##i "}"(W[i])
#define BQC(c) c "{v[176:179]}"(bq[0]), c "{v[180:183]}"(bq[1]), c "{v[184:187]}"(bq[2]), c "{v[188:191]}"(bq[3])
#define AC10(p) "a" #p "0", "a" #p "1", "a" #p "2", "a" #p "3", "a" #p "4", "a" #p "5", "a" #p "6", "a" #p "7", "a" #p "8", "a" #p "9"
#define ACLOB AC10(), AC10(1), AC10(2), AC10(3), AC10(4), AC10(5), AC10(6), AC10(7), AC10(8), AC10(9), AC10(10), AC10(11), AC10(12), AC10(13), AC10(14), \
              AC10(15), AC10(16), AC10(17), AC10(18), AC10(19), AC10(20), AC10(21), AC10(22), AC10(23), AC10(24), "a250", "a251", "a252", "a253", "a254", "a255"
struct St {
  v8u K[14];
  const char *kb, *vb;
  u32 lbase, tnext, rot;
};
#define PACE_ASM(tn, dt) ".Lp%=:\n\ts_memrealtime s[96:97]\n\ts_waitcnt lgkmcnt(0)\n\ts_sub_u32 s96, s96, " tn "\n\ts_cmp_lt_i32 s96, 0\n\ts_cbranch_scc1 .Lp%=\n\ts_add_u32 " tn ", " tn ", " dt "\n\ts_waitcnt vmcnt(4)\n\t"
template <int C> DI void issue_kop(St& s, u32 koff) {
  const char* base = s.kb + ((u32)(((C >> 2) & 1) * 131072 + (C >> 4) * 65536 + (C & 3) * 16384) ^ s.rot);
  if constexpr (k_in_lds(C)) {
    asm volatile("s_add_u32 m0, %2, %3\n\ts_nop 0\n\tglobal_load_lds_dwordx4 %0, %1 offset:%5 nt\n\ts_add_u32 m0, %2, %4\n\ts_nop 0\n\tglobal_load_lds_dwordx4 %0, %1 offset:%6 nt"
                 ::"v"(koff), "s"(base), "s"(s.lbase), "i"(k_lidx(C) * 2048 - k_imm(C)), "i"(k_lidx(C) * 2048 + 896 - k_imm(C)), "i"(k_imm(C)), "i"(k_imm(C) + 128)
                 : "memory", "scc", ACLOB);
  } else {
    constexpr int R = C - k_lidx(C);
#define KREG(i, lo, hi) if constexpr (R == i) asm volatile("global_load_dwordx4 v[" #lo ":" #lo "+3], %1, %2 offset:%3 nt\n\tglobal_load_dwordx4 v[" #lo "+4:" #hi "], %1, %2 offset:%4 nt" \
                                                           : "=&{v[" #lo ":" #hi "]}"(s.K[i]) : "v"(koff), "s"(base), "i"(k_imm(C)), "i"(k_imm(C) + 128) : "memory", ACLOB)
    KREG(0, 0, 7);
    KREG(1, 8, 15);
    KREG(2, 16, 23);
    KREG(3, 24, 31);
    KREG(4, 32, 39);
    KREG(5, 40, 47);
    KREG(6, 48, 55);
    KREG(7, 56, 63);
    KREG(8, 64, 71);
    KREG(9, 72, 79);
    KREG(10, 80, 87);
    KREG(11, 88, 95);
    KREG(12, 96, 103);
    KREG(13, 104, 111);
  }
}
template <int G> DI void issue_k(St& s, u32 koff) {
  if constexpr (G >= 4 && G != 12 && G != 13) asm volatile(PACE_ASM("%0", "%1") : "+s"(s.tnext) : "i"(G < 9 ? PACE1 : PACE2) : "s96", "s97", "scc", "memory");
  issue_kop<KORD[2 * G]>(s, koff); issue_kop<KORD[2 * G + 1]>(s, koff);
}
template <int J, bool GUARD> DI void issue_v(St& s, u32& vrun, u32 lo = 0, u32 cnt = 0) {
#define VLD PACE_ASM("%0", "%8") "v_xor_b32 v207, %7, %1\n\tglobal_load_dwordx4 a[%c6:%c6+3], v207, %2 nt\n\tglobal_load_dwordx4 a[%c6+4:%c6+7], v207, %3 nt\n\t" \
            "global_load_dwordx4 a[%c6+8:%c6+11], v207, %4 nt\n\tglobal_load_dwordx4 a[%c6+12:%c6+15], v207, %5 nt\n"
#define VIN "v"(vrun), "s"(s.vb), "s"(s.vb + 32768), "s"(s.vb + 65536), "s"(s.vb + 98304), "i"(16 * J), "i"((J & 7) * 256), "i"(J < 5 ? PACE2 : PACE3)
  if constexpr (GUARD)
    asm volatile("s_sub_u32 s96, %11, %9\n\ts_cmp_ge_u32 s96, %10\n\ts_cbranch_scc1 .Lv%=\n\t" VLD ".Lv%=:"
                 : "+s"(s.tnext) : VIN, "s"(lo), "s"(cnt), "i"(J) : "memory", "scc", "s96", "s97", "v207", ACLOB);
  else
    asm volatile(VLD : "+s"(s.tnext) : VIN : "memory", "scc", "s96", "s97", "v207", ACLOB);
}
DI void issue_v_range(St& s, u32& vrun, u32 lo, u32 hi) {
  const u32 cnt = hi - lo;
  issue_v<8, true>(s, vrun, lo, cnt); issue_v<9, true>(s, vrun, lo, cnt); issue_v<10, true>(s, vrun, lo, cnt); issue_v<11, true>(s, vrun, lo, cnt);
  issue_v<12, true>(s, vrun, lo, cnt); issue_v<13, true>(s, vrun, lo, cnt); issue_v<14, true>(s, vrun, lo, cnt); issue_v<15, true>(s, vrun, lo, cnt);
}
DI float lane_sel(float a, float b, u64 mask) { float r; asm("v_cndmask_b32 %0, %1, %2, %3" : "=v"(r) : "v"(a), "v"(b), "s"(mask)); return r; }
DI float diag4(const f32x4& t) {
  return lane_sel(lane_sel(t[0], t[1], 0xAAAAAAAAAAAAAAAAull), lane_sel(t[2], t[3], 0xAAAAAAAAAAAAAAAAull), 0xCCCCCCCCCCCCCCCCull);
}
DI float fmax_s(float a, float b) { float r; asm("v_max_f32 %0, %1, %2" : "=v"(r) : "v"(a), "v"(b)); return r; }

#define QK_ASM \
  "v_smfmac_f32_16x16x64_bf16 v[208:211], %20, v[224:231], %24\n\t" \
  "ds_read_b128 v[224:227], %25 offset:8192\n\t" \
  "ds_read_b128 v[228:231], %25 offset:9216\n\t" \
  "v_smfmac_f32_16x16x64_bf16 v[208:211], %21, %6, %24\n\t" \
  "v_smfmac_f32_16x16x64_bf16 v[208:211], %22, v[232:239], %24\n\t" \
  "ds_read_b128 v[232:235], %25 offset:10240\n\t" \
  "ds_read_b128 v[236:239], %25 offset:11264\n\t" \
  "v_smfmac_f32_16x16x64_bf16 v[208:211], %23, %7, %24\n\t" \
  "v_smfmac_f32_16x16x64_bf16 v[212:215], %20, v[240:247], %24\n\t" \
  "ds_read_b128 v[240:243], %25 offset:12288\n\t" \
  "ds_read_b128 v[244:247], %25 offset:13312\n\t" \
  "v_smfmac_f32_16x16x64_bf16 v[212:215], %21, v[248:255], %24\n\t" \
  "ds_read_b128 v[248:251], %25 offset:14336\n\t" \
  "ds_read_b128 v[252:255], %25 offset:15360\n\t" \
  "v_smfmac_f32_16x16x64_bf16 v[212:215], %22, %8, %24\n\t" \
  "s_waitcnt lgkmcnt(6)\n\t" \
  "v_smfmac_f32_16x16x64_bf16 v[212:215], %23, v[224:231], %24\n\t" \
  "ds_read_b128 v[224:227], %25 offset:16384\n\t" \
  "ds_read_b128 v[228:231], %25 offset:17408\n\t" \
  "v_add_f32_dpp v208, v210, v208 quad_perm:[1,0,3,2] row_mask:0xf bank_mask:0xf\n\t" \
  "v_smfmac_f32_16x16x64_bf16 v[216:219], %20, %9, %24\n\t" \
  "v_add_f32_dpp v209, v211, v209 quad_perm:[1,0,3,2] row_mask:0xf bank_mask:0xf\n\t" \
  "v_add_f32 %0, v208, v209\n\t" \
  "s_waitcnt lgkmcnt(6)\n\t" \
  "v_smfmac_f32_16x16x64_bf16 v[216:219], %21, v[232:239], %24\n\t" \
  "ds_read_b128 v[232:235], %25 offset:18432\n\t" \
  "ds_read_b128 v[236:239], %25 offset:19456\n\t" \
  "v_smfmac_f32_16x16x64_bf16 v[216:219], %22, %10, %24\n\t" \
  "v_mov_b64 v[208:209], 0\n\t" \
  "v_mov_b64 v[210:211], 0\n\t" \
  "s_waitcnt lgkmcnt(6)\n\t" \
  "v_smfmac_f32_16x16x64_bf16 v[216:219], %23, v[240:247], %24\n\t" \
  "ds_read_b128 v[240:243], %25 offset:20480\n\t" \
  "ds_read_b128 v[244:247], %25 offset:21504\n\t" \
  "v_add_f32_dpp v214, v212, v214 quad_perm:[1,0,3,2] row_mask:0xf bank_mask:0xf\n\t" \
  "s_waitcnt lgkmcnt(6)\n\t" \
  "v_smfmac_f32_16x16x64_bf16 v[220:223], %20, v[248:255], %24\n\t" \
  "ds_read_b128 v[248:251], %25 offset:22528\n\t" \
  "ds_read_b128 v[252:255], %25 offset:23552\n\t" \
  "v_add_f32_dpp v215, v213, v215 quad_perm:[1,0,3,2] row_mask:0xf bank_mask:0xf\n\t" \
  "v_smfmac_f32_16x16x64_bf16 v[220:223], %21, %11, %24\n\t" \
  "v_add_f32 v214, v214, v215\n\t" \
  "v_cndmask_b32 %0, %0, v214, %26\n\t" \
  "s_waitcnt lgkmcnt(6)\n\t" \
  "v_smfmac_f32_16x16x64_bf16 v[220:223], %22, v[224:231], %24\n\t" \
  "ds_read_b128 v[224:227], %25 offset:24576\n\t" \
  "ds_read_b128 v[228:231], %25 offset:25600\n\t" \
  "v_smfmac_f32_16x16x64_bf16 v[220:223], %23, %12, %24\n\t" \
  "v_mov_b64 v[212:213], 0\n\t" \
  "v_mov_b64 v[214:215], 0\n\t" \
  "v_add_f32_dpp v216, v218, v216 quad_perm:[1,0,3,2] row_mask:0xf bank_mask:0xf\n\t" \
  "s_waitcnt lgkmcnt(6)\n\t" \
  "v_smfmac_f32_16x16x64_bf16 v[208:211], %20, v[232:239], %24\n\t" \
  "ds_read_b128 v[232:235], %25 offset:26624\n\t" \
  "ds_read_b128 v[236:239], %25 offset:27648\n\t" \
  "v_smfmac_f32_16x16x64_bf16 v[208:211], %21, %13, %24\n\t" \
  "v_add_f32_dpp v217, v219, v217 quad_perm:[1,0,3,2] row_mask:0xf bank_mask:0xf\n\t" \
  "v_add_f32 %1, v216, v217\n\t" \
  "s_waitcnt lgkmcnt(6)\n\t" \
  "v_smfmac_f32_16x16x64_bf16 v[208:211], %22, v[240:247], %24\n\t" \
  "ds_read_b128 v[240:243], %25 offset:28672\n\t" \
  "ds_read_b128 v[244:247], %25 offset:29696\n\t" \
  "v_smfmac_f32_16x16x64_bf16 v[208:211], %23, %14, %24\n\t" \
  "v_mov_b64 v[216:217], 0\n\t" \
  "v_mov_b64 v[218:219], 0\n\t" \
  "v_add_f32_dpp v222, v220, v222 quad_perm:[1,0,3,2] row_mask:0xf bank_mask:0xf\n\t" \
  "s_waitcnt lgkmcnt(6)\n\t" \
  "v_smfmac_f32_16x16x64_bf16 v[212:215], %20, v[248:255], %24\n\t" \
  "ds_read_b128 v[248:251], %25 offset:30720\n\t" \
  "ds_read_b128 v[252:255], %25 offset:31744\n\t" \
  "v_add_f32_dpp v223, v221, v223 quad_perm:[1,0,3,2] row_mask:0xf bank_mask:0xf\n\t" \
  "s_waitcnt lgkmcnt(6)\n\t" \
  "v_smfmac_f32_16x16x64_bf16 v[212:215], %21, v[224:231], %24\n\t" \
  "ds_read_b128 v[224:227], %25 offset:32768\n\t" \
  "ds_read_b128 v[228:231], %25 offset:33792\n\t" \
  "v_smfmac_f32_16x16x64_bf16 v[212:215], %22, %15, %24\n\t" \
  "v_add_f32 v222, v222, v223\n\t" \
  "v_cndmask_b32 %1, %1, v222, %26\n\t" \
  "s_waitcnt lgkmcnt(6)\n\t" \
  "v_smfmac_f32_16x16x64_bf16 v[212:215], %23, v[232:239], %24\n\t" \
  "ds_read_b128 v[232:235], %25 offset:34816\n\t" \
  "ds_read_b128 v[236:239], %25 offset:35840\n\t" \
  "v_mov_b64 v[220:221], 0\n\t" \
  "v_smfmac_f32_16x16x64_bf16 v[216:219], %20, %16, %24\n\t" \
  "v_mov_b64 v[222:223], 0\n\t" \
  "v_add_f32_dpp v208, v210, v208 quad_perm:[1,0,3,2] row_mask:0xf bank_mask:0xf\n\t" \
  "s_waitcnt lgkmcnt(6)\n\t" \
  "v_smfmac_f32_16x16x64_bf16 v[216:219], %21, v[240:247], %24\n\t" \
  "v_add_f32_dpp v209, v211, v209 quad_perm:[1,0,3,2] row_mask:0xf bank_mask:0xf\n\t" \
  "v_add_f32 %2, v208, v209\n\t" \
  "v_smfmac_f32_16x16x64_bf16 v[216:219], %22, %17, %24\n\t" \
  "s_waitcnt lgkmcnt(4)\n\t" \
  "v_smfmac_f32_16x16x64_bf16 v[216:219], %23, v[248:255], %24\n\t" \
  "v_add_f32_dpp v214, v212, v214 quad_perm:[1,0,3,2] row_mask:0xf bank_mask:0xf\n\t" \
  "v_add_f32_dpp v215, v213, v215 quad_perm:[1,0,3,2] row_mask:0xf bank_mask:0xf\n\t" \
  "s_waitcnt lgkmcnt(2)\n\t" \
  "v_smfmac_f32_16x16x64_bf16 v[220:223], %20, v[224:231], %24\n\t" \
  "v_add_f32 v214, v214, v215\n\t" \
  "v_cndmask_b32 %2, %2, v214, %26\n\t" \
  "v_smfmac_f32_16x16x64_bf16 v[220:223], %21, %18, %24\n\t" \
  "s_waitcnt lgkmcnt(0)\n\t" \
  "v_smfmac_f32_16x16x64_bf16 v[220:223], %22, v[232:239], %24\n\t" \
  "v_smfmac_f32_16x16x64_bf16 v[220:223], %23, %19, %24\n\t" \
  "v_add_f32_dpp v216, v218, v216 quad_perm:[1,0,3,2] row_mask:0xf bank_mask:0xf\n\t" \
  "v_add_f32_dpp v217, v219, v217 quad_perm:[1,0,3,2] row_mask:0xf bank_mask:0xf\n\t" \
  "v_add_f32 %3, v216, v217\n\t" \
  "s_nop 4\n\t" \
  "v_add_f32_dpp v222, v220, v222 quad_perm:[1,0,3,2] row_mask:0xf bank_mask:0xf\n\t" \
  "v_add_f32_dpp v223, v221, v223 quad_perm:[1,0,3,2] row_mask:0xf bank_mask:0xf\n\t" \
  "v_add_f32 v222, v222, v223\n\t" \
  "v_cndmask_b32 %3, %3, v222, %26\n\t"
#define PV_ASM_A \
  "s_nop 1\n\t" \
  "v_and_b32_dpp v224, %5, %7 row_newbcast:0 row_mask:0xf bank_mask:0xf\n\t" \
  "v_and_b32_dpp v225, %5, %8 row_newbcast:0 row_mask:0xf bank_mask:0xf\n\t" \
  "v_and_b32_dpp v226, %6, %7 row_newbcast:0 row_mask:0xf bank_mask:0xf\n\t" \
  "v_and_b32_dpp v227, %6, %8 row_newbcast:0 row_mask:0xf bank_mask:0xf\n\t" \
  "v_and_b32_dpp v228, %5, %7 row_newbcast:2 row_mask:0xf bank_mask:0xf\n\t" \
  "v_and_b32_dpp v229, %5, %8 row_newbcast:2 row_mask:0xf bank_mask:0xf\n\t" \
  "v_smfmac_f32_16x16x64_bf16 %0, v[224:227], a[0:7], %9\n\t" \
  "v_and_b32_dpp v230, %6, %7 row_newbcast:2 row_mask:0xf bank_mask:0xf\n\t" \
  "v_and_b32_dpp v231, %6, %8 row_newbcast:2 row_mask:0xf bank_mask:0xf\n\t" \
  "v_smfmac_f32_16x16x64_bf16 %1, v[224:227], a[8:15], %9\n\t" \
  "v_and_b32_dpp v232, %5, %7 row_newbcast:4 row_mask:0xf bank_mask:0xf\n\t" \
  "v_and_b32_dpp v233, %5, %8 row_newbcast:4 row_mask:0xf bank_mask:0xf\n\t" \
  "v_add_f32_dpp %2, %2, %2 quad_perm:[1,0,3,2] row_mask:0xf bank_mask:0xf\n\t" \
  "v_smfmac_f32_16x16x64_bf16 %0, v[228:231], a[16:23], %9\n\t" \
  "v_and_b32_dpp v234, %6, %7 row_newbcast:4 row_mask:0xf bank_mask:0xf\n\t" \
  "v_and_b32_dpp v235, %6, %8 row_newbcast:4 row_mask:0xf bank_mask:0xf\n\t" \
  "v_smfmac_f32_16x16x64_bf16 %1, v[228:231], a[24:31], %9\n\t" \
  "v_and_b32_dpp v224, %5, %7 row_newbcast:6 row_mask:0xf bank_mask:0xf\n\t" \
  "v_and_b32_dpp v225, %5, %8 row_newbcast:6 row_mask:0xf bank_mask:0xf\n\t" \
  "v_add_f32_dpp %2, %2, %2 quad_perm:[2,3,0,1] row_mask:0xf bank_mask:0xf\n\t" \
  "v_smfmac_f32_16x16x64_bf16 %0, v[232:235], a[32:39], %9\n\t" \
  "v_and_b32_dpp v226, %6, %7 row_newbcast:6 row_mask:0xf bank_mask:0xf\n\t" \
  "v_and_b32_dpp v227, %6, %8 row_newbcast:6 row_mask:0xf bank_mask:0xf\n\t" \
  "v_smfmac_f32_16x16x64_bf16 %1, v[232:235], a[40:47], %9\n\t" \
  "v_and_b32_dpp v228, %5, %7 row_newbcast:8 row_mask:0xf bank_mask:0xf\n\t" \
  "v_and_b32_dpp v229, %5, %8 row_newbcast:8 row_mask:0xf bank_mask:0xf\n\t" \
  "v_add_f32_dpp %2, %2, %2 row_ror:4 row_mask:0xf bank_mask:0xf\n\t" \
  "v_smfmac_f32_16x16x64_bf16 %0, v[224:227], a[48:55], %9\n\t" \
  "v_and_b32_dpp v230, %6, %7 row_newbcast:8 row_mask:0xf bank_mask:0xf\n\t" \
  "v_and_b32_dpp v231, %6, %8 row_newbcast:8 row_mask:0xf bank_mask:0xf\n\t" \
  "v_smfmac_f32_16x16x64_bf16 %1, v[224:227], a[56:63], %9\n\t" \
  "v_and_b32_dpp v232, %5, %7 row_newbcast:10 row_mask:0xf bank_mask:0xf\n\t" \
  "v_and_b32_dpp v233, %5, %8 row_newbcast:10 row_mask:0xf bank_mask:0xf\n\t" \
  "v_add_f32_dpp %2, %2, %2 row_ror:8 row_mask:0xf bank_mask:0xf\n\t" \
  "v_mov_b32 %3, %2\n\t" \
  "v_smfmac_f32_16x16x64_bf16 %0, v[228:231], a[64:71], %9\n\t" \
  "v_and_b32_dpp v234, %6, %7 row_newbcast:10 row_mask:0xf bank_mask:0xf\n\t" \
  "v_and_b32_dpp v235, %6, %8 row_newbcast:10 row_mask:0xf bank_mask:0xf\n\t" \
  "v_smfmac_f32_16x16x64_bf16 %1, v[228:231], a[72:79], %9\n\t" \
  "v_and_b32_dpp v224, %5, %7 row_newbcast:12 row_mask:0xf bank_mask:0xf\n\t" \
  "v_and_b32_dpp v225, %5, %8 row_newbcast:12 row_mask:0xf bank_mask:0xf\n\t" \
  "v_permlane16_swap_b32 %2, %3\n\t" \
  "v_add_f32 %2, %2, %3\n\t" \
  "v_mov_b32 %3, %2\n\t"
#define PV_ASM_B \
  "v_smfmac_f32_16x16x64_bf16 %0, v[232:235], a[80:87], %9\n\t" \
  "v_and_b32_dpp v226, %6, %7 row_newbcast:12 row_mask:0xf bank_mask:0xf\n\t" \
  "v_and_b32_dpp v227, %6, %8 row_newbcast:12 row_mask:0xf bank_mask:0xf\n\t" \
  "v_smfmac_f32_16x16x64_bf16 %1, v[232:235], a[88:95], %9\n\t" \
  "v_and_b32_dpp v228, %5, %7 row_newbcast:14 row_mask:0xf bank_mask:0xf\n\t" \
  "v_and_b32_dpp v229, %5, %8 row_newbcast:14 row_mask:0xf bank_mask:0xf\n\t" \
  "v_permlane32_swap_b32 %2, %3\n\t" \
  "v_add_f32 %2, %2, %3\n\t"
#define PV_ASM_C \
  "v_smfmac_f32_16x16x64_bf16 %0, v[224:227], a[96:103], %9\n\t" \
  "v_and_b32_dpp v230, %6, %7 row_newbcast:14 row_mask:0xf bank_mask:0xf\n\t" \
  "v_and_b32_dpp v231, %6, %8 row_newbcast:14 row_mask:0xf bank_mask:0xf\n\t" \
  "v_smfmac_f32_16x16x64_bf16 %1, v[224:227], a[104:111], %9\n\t" \
  "v_and_b32_dpp v232, %5, %7 row_newbcast:1 row_mask:0xf bank_mask:0xf\n\t" \
  "v_and_b32_dpp v233, %5, %8 row_newbcast:1 row_mask:0xf bank_mask:0xf\n\t" \
  "v_smfmac_f32_16x16x64_bf16 %0, v[228:231], a[112:119], %9\n\t" \
  "v_and_b32_dpp v234, %6, %7 row_newbcast:1 row_mask:0xf bank_mask:0xf\n\t" \
  "v_and_b32_dpp v235, %6, %8 row_newbcast:1 row_mask:0xf bank_mask:0xf\n\t" \
  "v_smfmac_f32_16x16x64_bf16 %1, v[228:231], a[120:127], %9\n\t" \
  "v_and_b32_dpp v224, %5, %7 row_newbcast:3 row_mask:0xf bank_mask:0xf\n\t" \
  "v_and_b32_dpp v225, %5, %8 row_newbcast:3 row_mask:0xf bank_mask:0xf\n\t" \
  "v_smfmac_f32_16x16x64_bf16 %0, v[232:235], a[128:135], %9\n\t" \
  "v_and_b32_dpp v226, %6, %7 row_newbcast:3 row_mask:0xf bank_mask:0xf\n\t" \
  "v_and_b32_dpp v227, %6, %8 row_newbcast:3 row_mask:0xf bank_mask:0xf\n\t" \
  "v_smfmac_f32_16x16x64_bf16 %1, v[232:235], a[136:143], %9\n\t" \
  "v_and_b32_dpp v228, %5, %7 row_newbcast:5 row_mask:0xf bank_mask:0xf\n\t" \
  "v_and_b32_dpp v229, %5, %8 row_newbcast:5 row_mask:0xf bank_mask:0xf\n\t" \
  "v_smfmac_f32_16x16x64_bf16 %0, v[224:227], a[144:151], %9\n\t" \
  "v_and_b32_dpp v230, %6, %7 row_newbcast:5 row_mask:0xf bank_mask:0xf\n\t" \
  "v_and_b32_dpp v231, %6, %8 row_newbcast:5 row_mask:0xf bank_mask:0xf\n\t" \
  "v_smfmac_f32_16x16x64_bf16 %1, v[224:227], a[152:159], %9\n\t" \
  "v_and_b32_dpp v232, %5, %7 row_newbcast:7 row_mask:0xf bank_mask:0xf\n\t" \
  "v_and_b32_dpp v233, %5, %8 row_newbcast:7 row_mask:0xf bank_mask:0xf\n\t" \
  "v_smfmac_f32_16x16x64_bf16 %0, v[228:231], a[160:167], %9\n\t" \
  "v_and_b32_dpp v234, %6, %7 row_newbcast:7 row_mask:0xf bank_mask:0xf\n\t" \
  "v_and_b32_dpp v235, %6, %8 row_newbcast:7 row_mask:0xf bank_mask:0xf\n\t" \
  "v_smfmac_f32_16x16x64_bf16 %1, v[228:231], a[168:175], %9\n\t" \
  "v_and_b32_dpp v224, %5, %7 row_newbcast:9 row_mask:0xf bank_mask:0xf\n\t" \
  "v_and_b32_dpp v225, %5, %8 row_newbcast:9 row_mask:0xf bank_mask:0xf\n\t" \
  "v_smfmac_f32_16x16x64_bf16 %0, v[232:235], a[176:183], %9\n\t" \
  "v_and_b32_dpp v226, %6, %7 row_newbcast:9 row_mask:0xf bank_mask:0xf\n\t" \
  "v_and_b32_dpp v227, %6, %8 row_newbcast:9 row_mask:0xf bank_mask:0xf\n\t" \
  "v_smfmac_f32_16x16x64_bf16 %1, v[232:235], a[184:191], %9\n\t" \
  "v_and_b32_dpp v228, %5, %7 row_newbcast:11 row_mask:0xf bank_mask:0xf\n\t" \
  "v_and_b32_dpp v229, %5, %8 row_newbcast:11 row_mask:0xf bank_mask:0xf\n\t" \
  "s_waitcnt vmcnt(12)\n\t" \
  "v_smfmac_f32_16x16x64_bf16 %0, v[224:227], a[192:199], %9\n\t" \
  "v_and_b32_dpp v230, %6, %7 row_newbcast:11 row_mask:0xf bank_mask:0xf\n\t" \
  "v_and_b32_dpp v231, %6, %8 row_newbcast:11 row_mask:0xf bank_mask:0xf\n\t" \
  "v_smfmac_f32_16x16x64_bf16 %1, v[224:227], a[200:207], %9\n\t" \
  "v_and_b32_dpp v232, %5, %7 row_newbcast:13 row_mask:0xf bank_mask:0xf\n\t" \
  "v_and_b32_dpp v233, %5, %8 row_newbcast:13 row_mask:0xf bank_mask:0xf\n\t" \
  "s_waitcnt vmcnt(8)\n\t" \
  "v_smfmac_f32_16x16x64_bf16 %0, v[228:231], a[208:215], %9\n\t" \
  "v_and_b32_dpp v234, %6, %7 row_newbcast:13 row_mask:0xf bank_mask:0xf\n\t" \
  "v_and_b32_dpp v235, %6, %8 row_newbcast:13 row_mask:0xf bank_mask:0xf\n\t" \
  "v_smfmac_f32_16x16x64_bf16 %1, v[228:231], a[216:223], %9\n\t" \
  "v_and_b32_dpp v224, %5, %7 row_newbcast:15 row_mask:0xf bank_mask:0xf\n\t" \
  "v_and_b32_dpp v225, %5, %8 row_newbcast:15 row_mask:0xf bank_mask:0xf\n\t" \
  "s_waitcnt vmcnt(4)\n\t" \
  "v_smfmac_f32_16x16x64_bf16 %0, v[232:235], a[224:231], %9\n\t" \
  "v_and_b32_dpp v226, %6, %7 row_newbcast:15 row_mask:0xf bank_mask:0xf\n\t" \
  "v_and_b32_dpp v227, %6, %8 row_newbcast:15 row_mask:0xf bank_mask:0xf\n\t" \
  "v_smfmac_f32_16x16x64_bf16 %1, v[232:235], a[232:239], %9\n\t" \
  "s_nop 0\n\t" \
  "s_waitcnt vmcnt(0)\n\t" \
  "v_smfmac_f32_16x16x64_bf16 %0, v[224:227], a[240:247], %9\n\t" \
  "v_smfmac_f32_16x16x64_bf16 %1, v[224:227], a[248:255], %9\n\t"

#define RB0 "v[224:227]"
#define RB1 "v[228:231]"
#define RB2 "v[232:235]"
#define RB3 "v[236:239]"
#define RB4 "v[240:243]"
#define RB5 "v[244:247]"
#define RB6 "v[248:251]"
#define RB7 "v[252:255]"
#define RLD(BT, M, OFF) "s_mov_b64 exec, " M "\n\tglobal_load_dwordx4 " BT ", %[go], %[gb] offset:" OFF " sc1\n\t"
#define RSTEP(N, B0, BT, M, OFF, MFMAS) "s_waitcnt vmcnt(" N ")\n\tv_cmp_eq_u32_e64 " M ", -1, " B0 "\n\t" MFMAS RLD(BT, M, OFF) "s_mov_b64 exec, -1\n\t"
#define RHEAD(B0T, BTT, MT, OFFT, EXACT, ARM)                                                                                          \
  "s_mov_b32 s98, 0x20000\n\t" ARM "s_branch .Lissue%=\n"                                                                              \
  ".Lout%=:\n\ts_waitcnt vmcnt(0)\n\ts_branch .Lend%=\n"                                                                               \
  ".Lnan%=:\n\t" EXACT "v_cmp_eq_u32 vcc, -1, v223\n\ts_cbranch_vccz .Lout%=\n\ts_sub_u32 s98, s98, 1\n\ts_cbranch_scc1 .Lout%=\n\t"   \
  ARM "s_branch .Lissue%=\n"                                                                                                           \
  ".Lmiss%=:\n\ts_sub_u32 s98, s98, 1\n\ts_cbranch_scc1 .Lout%=\n\ts_cmp_lg_u32 %[rf], 0\n\ts_cbranch_scc1 .Lloop%=\n\ts_cmp_eq_u64 " MT ", %[full]\n\ts_cbranch_scc0 .Lloop%=\n\ts_sleep 2\n" \
  ".Lspin%=:\n\t" RLD(BTT, MT, OFFT) "s_mov_b64 exec, -1\n\ts_sub_u32 s98, s98, 1\n\ts_cbranch_scc1 .Lout%=\n\t"                        \
  "s_waitcnt vmcnt(1)\n\tv_cmp_eq_u32_e64 " MT ", -1, " B0T "\n\ts_cmp_eq_u64 " MT ", %[full]\n\ts_cbranch_scc1 .Lspin%=\n.Lissue%=:\n\t"
#define RTAIL "s_cbranch_scc1 .Lmiss%=\n\tv_cmp_u_f32 vcc, v216, v216\n\ts_cbranch_vccnz .Lnan%=\n.Lend%=:"
#define XMAX(a, b, c, d) "v_max3_u32 v223, v223, " a ", " b "\n\tv_max3_u32 v223, v223, " c ", " d "\n\t"
#define EXACT5 "v_mov_b32 v223, 0\n\t" XMAX("v224", "v225", "v226", "v227") XMAX("v228", "v229", "v230", "v231") XMAX("v232", "v233", "v234", "v235") \
               XMAX("v236", "v237", "v238", "v239") XMAX("v240", "v241", "v242", "v243")
#define EXACT8 EXACT5 XMAX("v244", "v245", "v246", "v247") XMAX("v248", "v249", "v250", "v251") XMAX("v252", "v253", "v254", "v255")
#define ARM5 "s_mov_b64 s[80:81], %[full]\n\ts_mov_b64 s[82:83], %[full]\n\ts_mov_b64 s[84:85], %[full]\n\ts_mov_b64 s[86:87], %[full]\n\ts_mov_b64 s[88:89], %[full]\n\t"
#define ARM8 ARM5 "s_mov_b64 s[90:91], %[full]\n\ts_cmp_gt_u32 %[nf], 6\n\ts_cselect_b64 s[92:93], -1, 0\n\ts_cmp_gt_u32 %[nf], 7\n\ts_cselect_b64 s[94:95], -1, 0\n\t"
#define OR5 "s_or_b64 s[96:97], s[80:81], s[82:83]\n\ts_or_b64 vcc, s[84:85], s[86:87]\n\ts_or_b64 s[96:97], s[96:97], vcc\n\ts_or_b64 s[96:97], s[96:97], s[88:89]\n\t"
#define CLOB5 "v223", "v224", "v225", "v226", "v227", "v228", "v229", "v230", "v231", "v232", "v233", "v234", "v235", "v236", "v237", "v238", "v239", \
              "v240", "v241", "v242", "v243", "s80", "s81", "s82", "s83", "s84", "s85", "s86", "s87", "s88", "s89", "s96", "s97", "s98", "vcc", "scc", "memory", ACLOB
#define CLOB8 CLOB5, "v244", "v245", "v246", "v247", "v248", "v249", "v250", "v251", "v252", "v253", "v254", "v255", "s90", "s91", "s92", "s93", "s94", "s95"
#define FILL4(a, b, c, d, v) "v_mov_b32 " a ", " v "\n\tv_mov_b32 " b ", " v "\n\tv_mov_b32 " c ", " v "\n\tv_mov_b32 " d ", " v "\n\t"
#define RING5(PRE, O0, O1, O2, O3, O4, MM0, MM1, MM2, MM3, MM4)                                                          \
  PRE RHEAD("v224", RB0, "s[80:81]", O0, EXACT5, ARM5) RLD(RB0, "s[80:81]", O0) RLD(RB1, "s[82:83]", O1) RLD(RB2, "s[84:85]", O2) \
  RLD(RB3, "s[86:87]", O3) RLD(RB4, "s[88:89]", O4) "s_mov_b64 exec, -1\n.Lloop%=:\n\t"                                                  \
  RSTEP("4", "v224", RB0, "s[80:81]", O0, MM0) RSTEP("4", "v228", RB1, "s[82:83]", O1, MM1) RSTEP("4", "v232", RB2, "s[84:85]", O2, MM2) \
  RSTEP("4", "v236", RB3, "s[86:87]", O3, MM3)                                                                                          \
  "s_or_b64 s[96:97], s[80:81], s[82:83]\n\ts_or_b64 vcc, s[84:85], s[86:87]\n\ts_or_b64 s[96:97], s[96:97], vcc\n\t"                   \
  RSTEP("4", "v240", RB4, "s[88:89]", O4, MM4) "s_nop 0\n\ts_or_b64 s[96:97], s[96:97], s[88:89]\n\t" RTAIL

DI u32 poll_dword(const char* base, u32 off) {
  u32 v;
#pragma unroll 1
  for (int it = 0; it < SPIN_CAP; it++) {
    asm volatile("global_load_dword %0, %1, %2 sc1\n\ts_waitcnt vmcnt(0)" : "=&v"(v) : "v"(off), "s"(base) : "memory");
    if (!__any(v == SENT)) break;
  }
  return v;
}
__global__ void __launch_bounds__(256, 1)
mega(const char* x, const char* kc, const char* vc, const char* wq, const char* wo, const char* w1, const char* b1,
     const char* w2, const char* b2, char* out, char* ws_raw) {
  int w_ = __builtin_amdgcn_readfirstlane(threadIdx.x >> 6), wg_ = blockIdx.x;
#define ROLES()                                                                                                             \
  OPAQUE(w_); OPAQUE(wg_);                                                                                                  \
  const int w = w_, wg = wg_, xg = wg & 7, r = wg >> 3, sub = r & 3, b = xg, h = r, grp = (r >> 2) * 4 + (xg & 3);          \
  const bool isA = xg >= 4, isC = !isA && grp < 20, isE = !isA && grp >= 20 && grp < 30, isD = isA && grp < 27;             \
  const int aidx = (xg & 3) * 32 + r, fidx = grp * 4 + sub, kh = grp >= 10 ? 1 : 0, cs = (isC ? grp - 10 * kh : grp - 20) * 4 + sub; \
  const u32 csoff = (u32)(cs / 10) * CH, csr = cs % 10;                                                                      \
  (void)b; (void)h; (void)isD; (void)isE; (void)aidx; (void)fidx; (void)csoff; (void)csr; (void)w
  const char* kc0;
  asm volatile("s_load_dwordx2 %0, %1, 0x8" : "=s"(kc0) : "s"(__builtin_amdgcn_kernarg_segment_ptr()) : "memory");
  char* ws;
  St s;
  s.lbase = LDS_K + w_ * LDS_WAVE;
  u32 kvoff = (u32)((wg_ & 7) * 32 + (wg_ >> 3)) * 1048576u + 786432u + w_ * 4096;
  s.rot = (u32)((((wg_ >> 3) ^ (wg_ >> 5)) & 1) << 14 | ((wg_ >> 2) & 1) << 17);
  const u32 rho = (u32)(((wg_ >> 5) & 1) << 8 | ((wg_ >> 3) & 1) * 0x600);
  u32 tag, aux;
  v4u B[5], W[16];
  {
    LANES();
    u32 pf = 0;
    if (w_ == 2) {
      u64 pc; u32 len;
      asm volatile("s_getpc_b64 %0\n.Lpc%=:\n\ts_mov_b32 %1, .Lmega_end-.Lpc%=" : "=s"(pc), "=s"(len));
      const u32 po = min((u32)((wg_ & ~7) + (lane & 7)) * 128u, len - 4u) & ~3u;
      asm volatile("global_load_dword %0, %1, %2" : "=v"(pf) : "v"(po), "s"(pc) : "memory");
    }
    u32 koff = ((n >> 1) * 256 + g * 32 + (n & 1) * 16) ^ rho;
    asm volatile("s_waitcnt lgkmcnt(0)" : "+s"(kc0), "+s"(kvoff), "+s"(s.lbase), "+v"(koff)::"memory");
    s.kb = kc0 + kvoff;
    issue_k<0>(s, koff);
    issue_k<1>(s, koff);
    GAP();
    OPAQUE(ws_raw); OPAQUE(vc);
    ws = (char*)(((size_t)ws_raw + 0x1FFFFF) & ~(size_t)0x1FFFFF);
    s.vb = vc + kvoff;
    ROLES();
    u32 epoch;
    {
      const u32 z = 0;
      if (w == 1) asm volatile("global_load_dword %0, %1, %2 nt" : "=v"(pf) : "v"(z), "s"(s.vb) : "memory");
      asm volatile("global_load_dword %0, %1, %2 sc1" : "=v"(epoch) : "v"(z), "s"(ws) : "memory");
      const char* ap = isD ? b1 + fidx * 32 : isE ? b2 + cs * 32 : isC ? x + cs * 32 : x;
      const u32 ao = ecol * 2 + (isC ? eb * 1280 : 0);
      asm volatile("global_load_dword %0, %1, %2" : "=v"(aux) : "v"(ao), "s"(ap) : "memory");
    }
    const u32 own = WS_Q + Q_L + (wg >> 3) * CH + (wg & 7) * 1024;
    if (w == 3) {
      const u32 ro = own + lane * 16;
      const v4u sv = {SENT, SENT, SENT, SENT};
      asm volatile("global_store_dwordx4 %0, %1, %2 sc1\n\tglobal_store_dwordx4 %0, %1, %3 sc1\n\ts_nop 1" ::"v"(ro), "v"(sv), "s"(ws), "s"(ws + SIDE) : "memory");
      if (wg < (RESET_CHUNKS - 32) * 8)
        asm volatile("global_store_dwordx4 %0, %1, %2 sc1\n\tglobal_store_dwordx4 %0, %1, %3 sc1\n\ts_nop 1" ::"v"(ro), "v"(sv), "s"(ws + 32 * CH), "s"(ws + 32 * CH + SIDE) : "memory");
      if (isA && lane < 32) {
        const u32 qo = WS_Q + q_off(eb, aidx) + ec * 16;
        asm volatile("global_store_dwordx4 %0, %1, %2 sc1\n\ts_nop 1" ::"v"(qo), "v"(sv), "s"(ws) : "memory");
      }
      if (r >= VOTE_R && lane < 2) st_sc1(ws, WS_CAL + (wg & 31) * 128 + lane * SIDE, 0u);
      if (isE) st_sc1(out, eb * 1280 + cs * 32 + ecol * 2, 0u);
    }
#pragma unroll
    for (int i = 0; i < 5; i++) B[i] = v4u{ONES, ONES, ONES, ONES};
    if (isA) {
      const u32 xo = (n & 7) * 1280 + g * 16;
      const char* xb = x + w * 320;
      u64 sv; const u64 mk = 0x00FF00FF00FF00FFull;
      asm volatile("s_mov_b64 %5, exec\n\ts_mov_b64 exec, %8\n\t"
                   "global_load_dwordx4 %0, %6, %7\n\tglobal_load_dwordx4 %1, %6, %7 offset:64\n\tglobal_load_dwordx4 %2, %6, %7 offset:128\n\t"
                   "global_load_dwordx4 %3, %6, %7 offset:192\n\tglobal_load_dwordx4 %4, %6, %7 offset:256\n\ts_mov_b64 exec, %5"
                   : "+v"(B[0]), "+v"(B[1]), "+v"(B[2]), "+v"(B[3]), "+v"(B[4]), "=&s"(sv) : "v"(xo), "s"(xb), "s"(mk) : "memory");
    }
    const char* wb = isA ? wq + (size_t)(w * 160) * 8192 + aidx * 64 : isC ? wo + (size_t)(kh * 2048 + w * 512) * 1280 + cs * 32 : w2 + (size_t)(w * 448) * 1280 + cs * 32;
    u32 wo_ = isA ? (lane >> 2) * 8192 + (lane & 3) * 16 : (lane >> 1) * 1280 + (lane & 1) * 16;
    u32 wstep = isA ? 16 * 8192 : 32 * 1280;
    int wn = isA ? 10 : isC ? 16 : isE ? (w == 3 ? 12 : 14) : 0;
    const u32 stg = w * 2048;
    const int q4 = (lane >> 2) & 3, p4 = lane & 3;
    u32 ta = stg + (isA ? (8 * g + q4) * 64 + 8 * p4 : (8 * (p4 >> 1) + 16 * g + q4) * 32 + 8 * (p4 & 1)), td1 = isA ? 32 : 16, td2 = isA ? 256 : 128;
#define W_LOAD(i) asm volatile("s_cmp_lt_i32 %2, 1\n\ts_cbranch_scc1 .Lw%=\n\tglobal_load_dwordx4 %0, %1, %3\n\tv_add_u32 %1, %4, %1\n.Lw%=:\n\ts_sub_i32 %2, %2, 1" \
                               : WC("+", i), "+v"(wo_), "+s"(wn) : "s"(wb), "s"(wstep) : "memory", "scc")
#define W_TURN(i, j) asm volatile("ds_write_b128 %2, %0\n\tds_write_b128 %2, %1 offset:1024\n\tds_read_b64_tr_b16 " WL##i ", %3\n\tds_read_b64_tr_b16 " WH##i ", %4\n\t" \
                                  "ds_read_b64_tr_b16 " WL##j ", %5\n\tds_read_b64_tr_b16 " WH##j ", %6"                                                          \
                                  : WC("+", i), WC("+", j) : "v"(tw), "v"(ta), "v"(ta + td2), "v"(ta + td1), "v"(ta + td1 + td2) : "memory")
#define W_DONE() asm volatile("s_waitcnt lgkmcnt(0)" : WC("+", 0), WC("+", 1), WC("+", 2), WC("+", 3), WC("+", 4), WC("+", 5), WC("+", 6), WC("+", 7), WC("+", 8), WC("+", 9), WC("+", 10), WC("+", 11), WC("+", 12), WC("+", 13), WC("+", 14), WC("+", 15) :: "memory")
    const u32 tw = stg + lane * 16;
    asm volatile("" : WC("=", 0), WC("=", 1), WC("=", 2), WC("=", 3), WC("=", 4), WC("=", 5), WC("=", 6), WC("=", 7), WC("=", 8), WC("=", 9), WC("=", 10), WC("=", 11), WC("=", 12), WC("=", 13), WC("=", 14), WC("=", 15));
    u32 p1 = 0, p2 = 0;
    W_LOAD(0); W_LOAD(1);
    if (w == 3 && r >= VOTE_R) asm volatile("s_load_dword %0, %1, 0x200" : "+s"(p1) : "s"(ws) : "memory");
    issue_k<2>(s, koff);
    W_LOAD(2);
    issue_k<3>(s, koff);
    u32 tst = (u32)__builtin_amdgcn_s_memrealtime();
    s.tnext = tst + PACE_OFF;
    W_LOAD(3); issue_k<4>(s, koff); W_LOAD(4);
    issue_k<5>(s, koff); W_LOAD(5); issue_k<6>(s, koff); W_LOAD(6);
    if (w == 3) {
      asm volatile("s_waitcnt vmcnt(0) lgkmcnt(0)" : "+v"(epoch), "+s"(p1)::"memory");
      const u32 tag = (__builtin_amdgcn_readfirstlane(epoch) + 2u) & ~1u;
      u32 vote = 0;
      if (r >= VOTE_R) {
      const u32 o1 = WS_CAL + (wg & 31) * 128 + (r & 1) * SIDE, o2 = o1 ^ SIDE;
      u64 t0, t1, t2;
      asm volatile("s_memtime %0\n\ts_load_dword %3, %4, %5\n\ts_waitcnt lgkmcnt(0)\n\ts_memtime %1\n\ts_load_dword %3, %4, %6\n\ts_waitcnt lgkmcnt(0)\n\t"
                   "s_memtime %2\n\ts_waitcnt lgkmcnt(0)"
                   : "=&s"(t0), "=&s"(t1), "=&s"(t2), "=&s"(p2) : "s"(ws), "s"(o1), "s"(o2) : "memory");
      const u32 d1 = (u32)t1 - (u32)t0, d2 = (u32)t2 - (u32)t1;
      vote = ((d1 < d2 ? (r & 1) : 1 - (r & 1)) ^ (xg >> 2)) & 1;
      }
      if (lane == 0) st2_sc1(ws, WS_FLAGS + wg * 8, v2u{tag | vote, tst});
    }
    issue_k<7>(s, koff); W_LOAD(7); issue_k<8>(s, koff); W_LOAD(8); W_LOAD(9);
    issue_k<9>(s, koff); issue_k<10>(s, koff); issue_k<11>(s, koff);
    asm volatile("s_waitcnt vmcnt(12)" : "+v"(epoch), "+v"(aux), "+v"(pf), "+v"(B[0]), "+v"(B[1]), "+v"(B[2]), "+v"(B[3]), "+v"(B[4])::"memory");
    tag = (__builtin_amdgcn_readfirstlane(epoch) + 2u) & ~1u;
    W_TURN(0, 1); W_TURN(2, 3); W_TURN(4, 5); W_TURN(6, 7); W_TURN(8, 9); W_DONE();
    if (isA) {
      wb = w1 + (size_t)(w * 160) * 3456 + fidx * 32; wo_ = (lane >> 1) * 3456 + (lane & 1) * 16; wstep = 32 * 3456; wn = isD ? 5 : 0;
    }
    if (isC && kh) aux = 0;
    const int rk = __builtin_amdgcn_readfirstlane(isD ? 1 : isC ? 2 + kh : isE ? 4 + (cs == 0) : 0), nf = __builtin_amdgcn_readfirstlane(isC ? 8 : w != 3 ? 7 : 6);
    u32 pubo = isD ? WS_H + (fidx / 28) * CH + (fidx % 28) * 256 : isC ? WS_XP + csoff + SIDE + csr * 512 + kh * 256 : WS_X2 + csoff + SIDE + csr * 256;
    u32 gbo = isD ? WS_XP + w * CH + SIDE + 2048 : (isC ? WS_O + (kh * 4 + w) * CH : WS_H + w * CH) + 4096;
    u32 rpo = csoff + (isC ? WS_X2 + csr * 256 : WS_XP + csr * 512);
    char* outp = out + cs * 32;
    u32 qbo = WS_Q + q_off(b, h * 4), opo = WS_O + (h >> 2) * CH + (h & 3) * 2048 + b * 32;
    OPAQUE(qbo); OPAQUE(opo);
    OPAQUE(pubo); OPAQUE(gbo); OPAQUE(rpo); OPAQUE(outp);
    const int rf = __builtin_amdgcn_readfirstlane(r == 20 || r == 24);
    u32 side = 0;

#pragma unroll 1
    for (int l = 0; l < 2; l++) {
      const bool first = l == 0;
      OPAQUE(ws);
      ROLES();
      if (isA) {
        LANES();
        u32 wa = n < 8 ? SC_ROLE + w * 1024 + (g >> 1) * 256 + n * 32 + (g & 1) * 16 : n == 8 ? SC_CS + w * 128 + g * 16 : SC_DUMP + lane * 16;
        u32 wa2 = wa + (n < 8 ? 512 : n == 8 ? 64 : 0);
        u32 sa = n < 8 && (g == 2 || g == (n >> 2)) ? SC_STAT + w * 64 + n * 8 + (g == 2 ? 4 : 0) : SC_DUMP + lane * 4;
        const int qb_ = lane >> 3, qc = (lane >> 1) & 1, qp2 = (lane & 1) * 2 + ((lane >> 2) & 1);
        u32 qo = (q_off(qb_, 0) ^ side) + w * 32 + (lane & 7) * 4;
        u32 ra = qc * 512 + w * 256 + qb_ * 32 + qp2 * 8, rc = qc * 64 + w * 32 + qp2 * 8, rs_ = qb_ * 8;
        char* qp = ws + WS_Q + l * Q_L + q_off(0, aidx);
        PIN(wa); PIN(wa2); PIN(sa); PIN(qo); rc += SC_CS; rs_ += SC_STAT;
        PIN(ra); PIN(rc); PIN(rs_); OPAQUE(qp);
        f32x4 t0, t1, gr;
        if (COLD(first)) {
          asm volatile("s_nop 1\n\t" MF "%0, %3, %13, 0\n\t" MF "%1, %4, %13, 0\n\t" MF "%2, %13, %13, 0\n\t"
                       MF "%0, %5, %14, %0\n\t" MF "%1, %6, %14, %1\n\t" MF "%2, %14, %14, %2\n\t"
                       MF "%0, %7, %15, %0\n\t" MF "%1, %8, %15, %1\n\t" MF "%2, %15, %15, %2\n\t"
                       MF "%0, %9, %16, %0\n\t" MF "%1, %10, %16, %1\n\t" MF "%2, %16, %16, %2\n\t"
                       MF "%2, %17, %17, %2\n\t" MF "%0, %11, %17, %0\n\t" MF "%1, %12, %17, %1\n\ts_nop 7"
                       : "=&{v[216:219]}"(t0), "=&v"(t1), "=&v"(gr)
                       : WC("", 0), WC("", 1), WC("", 2), WC("", 3), WC("", 4), WC("", 5), WC("", 6), WC("", 7), WC("", 8), WC("", 9),
                         "v"(B[0]), "v"(B[1]), "v"(B[2]), "v"(B[3]), "v"(B[4]));
        } else {
          const u32 xo = g * 128 + (n & 7) * 16;
          const char* xb = ws + WS_X2 + w * CH + (SIDE ^ side);
          const u64 mk = 0x00FF00FF00FF00FFull;
#define AMM(B, WA, WB, C0, C1, C2) MF "%0, " WA ", " B ", " C0 "\n\t" MF "%2, " B ", " B ", " C2 "\n\t" MF "%1, " WB ", " B ", " C1 "\n\t"
          asm volatile(RING5("v_mov_b32 v223, 0x3f803f80\n\t" FILL4("v224", "v225", "v226", "v227", "v223") FILL4("v228", "v229", "v230", "v231", "v223")
                             FILL4("v232", "v233", "v234", "v235", "v223") FILL4("v236", "v237", "v238", "v239", "v223") FILL4("v240", "v241", "v242", "v243", "v223"),
                             "0", "512", "1024", "1536", "2048",
                             AMM(RB0, "%3", "%4", "0", "0", "0"), AMM(RB1, "%5", "%6", "%0", "%1", "%2"), AMM(RB2, "%7", "%8", "%0", "%1", "%2"),
                             AMM(RB3, "%9", "%10", "%0", "%1", "%2"), AMM(RB4, "%11", "%12", "%0", "%1", "%2"))
                       : "=&{v[216:219]}"(t0), "=&v"(t1), "=&v"(gr)
                       : WC("", 0), WC("", 1), WC("", 2), WC("", 3), WC("", 4), WC("", 5), WC("", 6), WC("", 7), WC("", 8), WC("", 9),
                         [go] "v"(xo), [gb] "s"(xb), [full] "s"(mk), [rf] "s"(rf)
                       : CLOB5);
        }
        lds_st<f32x4>(wa, t0);
        lds_st<f32x4>(wa2, t1);
        lds_st<float>(sa, lane_sel(diag4(gr), gr[0], 0x0000FFFF00000000ull));
        if (COLD(first)) { OPAQUE(s.kb); issue_k<12>(s, koff); issue_k<13>(s, koff); }
        lds_barrier();
        if (w < 2) {
          f32x2 s4[4], a4[4], c4[4];
#pragma unroll
          for (int i = 0; i < 4; i++) s4[i] = lds_ld<f32x2>(rs_ + i * 64);
#pragma unroll
          for (int i = 0; i < 4; i++) a4[i] = lds_ld<f32x2>(ra + SC_ROLE + i * 1024);
#pragma unroll
          for (int i = 0; i < 4; i++) c4[i] = lds_ld<f32x2>(rc + i * 128);
          GAP();
          const f32x2 st = (s4[0] + s4[1]) + (s4[2] + s4[3]), acc = (a4[0] + a4[1]) + (a4[2] + a4[3]), csum = (c4[0] + c4[1]) + (c4[2] + c4[3]);
          const float mean = st[1] * (1.f / 640.f), var = fmax_s(st[0] * (1.f / 640.f) - mean * mean, 0.f);
          const float rs = __builtin_amdgcn_rsqf(var + 1e-5f);
          st_sc1(qp, qo, min(pk_bf16(rs * (acc[0] - mean * csum[0]), rs * (acc[1] - mean * csum[1])), SENT - 1));
        }
        NAP_IF((w >> 1) & l, QUIET);
      }
      LANES();
      OPAQUE(s.lbase);
      const u32 qoff = g * 128 + (n & 3) * 16;
      const char* qb = ws + (qbo ^ side);
      u32 vrun = ((g >> 1) * 16384 + (g & 1) * 2048 + n * 16) ^ (s.rot | rho), vhi = 16;
      v4u bq[4];
      asm volatile("v_mov_b64 v[176:177], 0\n\tv_mov_b64 v[178:179], 0\n\tv_mov_b64 v[180:181], 0\n\tv_mov_b64 v[182:183], 0\n\t"
                   "v_mov_b64 v[184:185], 0\n\tv_mov_b64 v[186:187], 0\n\tv_mov_b64 v[188:189], 0\n\tv_mov_b64 v[190:191], 0" : BQC("="));
#define Q_LOADS "s_mov_b64 exec, %6\n\tglobal_load_dwordx4 %0, %4, %5 sc1\n\ts_lshl_b64 exec, exec, 4\n\tglobal_load_dwordx4 %1, %4, %5 sc1\n\t" \
                "s_lshl_b64 exec, exec, 4\n\tglobal_load_dwordx4 %2, %4, %5 sc1\n\ts_lshl_b64 exec, exec, 4\n\tglobal_load_dwordx4 %3, %4, %5 sc1\n\ts_mov_b64 exec, -1"
#define Q_OPS : BQC("+") : "v"(qoff), "s"(qb), "s"(0x000F000F000F000Full) : "memory", "scc"
#define Q_MAX(a, c) "v_max3_u32 " a ", v176, v177, v178\n\tv_max3_u32 " c ", v184, v185, v186\n\tv_max3_u32 " a ", " a ", v179, v180\n\tv_max3_u32 " c ", " c ", v187, v188\n\t" \
                    "v_max3_u32 " a ", " a ", v181, v182\n\tv_max3_u32 " c ", " c ", v189, v190\n\tv_max3_u32 " a ", " a ", v183, v191\n\tv_max_u32 " a ", " a ", " c
      auto q_missing = [&]() {
        u32 a, c;
        asm volatile(Q_MAX("%0", "%1") : "=&v"(a), "=&v"(c) : BQC(""));
        return __any(a == SENT) != 0;
      };
#define Q_MISSING() q_missing()
      bool missing = true;
      if (COLD(first)) {
        OPAQUE(s.kb); OPAQUE(s.vb); OPAQUE(s.rot);
        W_LOAD(10);
        if (!isA) { issue_k<12>(s, koff); issue_k<13>(s, koff); }
        s.tnext += 2 * PACE2;
        issue_k<14>(s, koff); W_LOAD(11); issue_k<15>(s, koff);
        v4u fl, fm;
        const u32 fo = lane * 32;
        const char* fb = ws + WS_FLAGS;
        asm volatile("global_load_dwordx4 %0, %2, %3 sc1\n\tglobal_load_dwordx4 %1, %2, %3 offset:16 sc1" : "=&{v[224:227]}"(fl), "=&{v[228:231]}"(fm) : "v"(fo), "s"(fb) : "memory");
        issue_v<0, false>(s, vrun); W_LOAD(12); issue_v<1, false>(s, vrun); W_LOAD(13); issue_v<2, false>(s, vrun);
        int votes, dref;
        asm volatile("s_waitcnt vmcnt(12)\n\ts_mov_b32 s98, 0x20000\n.Lf%=:\n\tv_xor_b32 v232, %4, v224\n\tv_xor_b32 v233, %4, v226\n\tv_xor_b32 v234, %4, v228\n\t"
                     "v_xor_b32 v235, %4, v230\n\tv_or3_b32 v236, v232, v233, v234\n\tv_or_b32 v236, v236, v235\n\tv_cmp_lt_u32 vcc, 1, v236\n\ts_cbranch_vccz .Lg%=\n\t"
                     "global_load_dwordx4 v[224:227], %6, %7 sc1\n\tglobal_load_dwordx4 v[228:231], %6, %7 offset:16 sc1\n\ts_waitcnt vmcnt(0)\n\t"
                     "s_sub_u32 s98, s98, 1\n\ts_cbranch_scc0 .Lf%=\n.Lg%=:\n\t"
                     "v_cmp_eq_u32 vcc, 1, v232\n\ts_bcnt1_i32_b64 %2, vcc\n\tv_cmp_eq_u32 vcc, 1, v233\n\ts_bcnt1_i32_b64 s98, vcc\n\ts_add_u32 %2, %2, s98\n\t"
                     "v_cmp_eq_u32 vcc, 1, v234\n\ts_bcnt1_i32_b64 s98, vcc\n\ts_add_u32 %2, %2, s98\n\tv_cmp_eq_u32 vcc, 1, v235\n\ts_bcnt1_i32_b64 s98, vcc\n\ts_add_u32 %2, %2, s98\n\t"
                     "v_subrev_u32 v232, %5, v225\n\tv_subrev_u32 v233, %5, v227\n\tv_subrev_u32 v234, %5, v229\n\tv_subrev_u32 v235, %5, v231\n\t"
                     "v_mov_b32 v236, 0x190\n\tv_mov_b32 v224, 0xfffffe70\n\tv_med3_i32 v232, v232, v224, v236\n\tv_med3_i32 v233, v233, v224, v236\n\t"
                     "v_med3_i32 v234, v234, v224, v236\n\tv_med3_i32 v235, v235, v224, v236\n\tv_add3_u32 v232, v232, v233, v234\n\tv_add_u32 v232, v232, v235\n\ts_nop 1\n\t"
                     "v_add_u32_dpp v232, v232, v232 quad_perm:[1,0,3,2] row_mask:0xf bank_mask:0xf\n\ts_nop 1\n\t"
                     "v_add_u32_dpp v232, v232, v232 quad_perm:[2,3,0,1] row_mask:0xf bank_mask:0xf\n\ts_nop 1\n\t"
                     "v_add_u32_dpp v232, v232, v232 row_ror:4 row_mask:0xf bank_mask:0xf\n\ts_nop 1\n\t"
                     "v_add_u32_dpp v232, v232, v232 row_ror:8 row_mask:0xf bank_mask:0xf\n\ts_nop 1\n\t"
                     "v_readlane_b32 %3, v232, 0\n\tv_readlane_b32 s96, v232, 16\n\tv_readlane_b32 s97, v232, 32\n\tv_readlane_b32 s98, v232, 48\n\t"
                     "s_add_i32 %3, %3, s96\n\ts_add_i32 s97, s97, s98\n\ts_add_i32 %3, %3, s97\n\ts_ashr_i32 %3, %3, 8"
                     : "+{v[224:227]}"(fl), "+{v[228:231]}"(fm), "=&s"(votes), "=&s"(dref)
                     : "s"(tag), "s"(tst), "v"(fo), "s"(fb)
                     : "v232", "v233", "v234", "v235", "v236", "s96", "s97", "s98", "vcc", "scc", "memory");
        side = votes > (32 - VOTE_R) * 4 ? SIDE : 0;
        s.tnext += dref;
        issue_v<3, false>(s, vrun); W_LOAD(14); issue_v<4, false>(s, vrun); W_LOAD(15);
        asm volatile(Q_LOADS Q_OPS);
        issue_v<5, false>(s, vrun); issue_v<6, false>(s, vrun); issue_v<7, false>(s, vrun);
        vhi = 8 + w;
        vrun ^= 131072;
        issue_v<8, true>(s, vrun, 8, w); issue_v<9, true>(s, vrun, 8, w); issue_v<10, true>(s, vrun, 8, w);
        asm volatile("s_waitcnt vmcnt(12)" : BQC("+")::"memory");
        missing = Q_MISSING();
        {
        LANES(); ROLES();
        const int q4 = (lane >> 2) & 3, p4 = lane & 3;
        const u32 stg = w * 2048, tw = stg + lane * 16, td1 = isA ? 1024 : 16, td2 = 128;
        const u32 ta = stg + (isA ? (8 * g + q4) * 32 + 8 * p4 : (8 * (p4 >> 1) + 16 * g + q4) * 32 + 8 * (p4 & 1));
        W_TURN(10, 11); W_TURN(12, 13); W_TURN(14, 15); W_DONE();
        asm volatile("s_cmp_eq_u32 %4, 1\n\ts_cbranch_scc0 .La%=\n\tv_mov_b32 v172, 0x3f803f80\n\tv_mov_b32 v173, v172\n\tv_mov_b32 v174, v172\n\tv_mov_b32 v175, v172\n.La%=:\n\t"
                     "s_cmp_lt_u32 %4, 4\n\ts_cbranch_scc1 .Lb%=\n\tv_mov_b64 " WL14 ", 0\n\tv_mov_b64 " WH14 ", 0\n\tv_mov_b64 " WL15 ", 0\n\tv_mov_b64 " WH15 ", 0\n\t"
                     "s_cmp_eq_u32 %5, 6\n\ts_cbranch_scc0 .Lb%=\n\tv_mov_b64 " WL12 ", 0\n\tv_mov_b64 " WH12 ", 0\n\tv_mov_b64 " WL13 ", 0\n\tv_mov_b64 " WH13 ", 0\n.Lb%=:"
                     : WC("+", 12), WC("+", 13), WC("+", 14), WC("+", 15) : "s"(rk), "s"(nf) : "scc");
        if (rk == 1) {
          f32x4 c1;
          asm volatile("s_nop 1\n\t" MF "%0, %1, %6, 0\n\t" MF "%0, %2, %6, %0\n\t" MF "%0, %3, %6, %0\n\t" MF "%0, %4, %6, %0\n\t" MF "%0, %5, %6, %0\n\ts_nop 7"
                       : "=&v"(c1) : WC("", 10), WC("", 11), WC("", 12), WC("", 13), WC("", 14), WC("", 15));
          if (n == 0) lds_st<f32x4>(SC_CSD + w * 64 + g * 16, c1);
        }
        }
        if (missing) {
          asm volatile(Q_LOADS Q_OPS);
          issue_v_range(s, vrun, vhi, vhi + 3);
          vhi += 3;
          asm volatile("s_waitcnt vmcnt(12)" : BQC("+")::"memory");
          missing = Q_MISSING();
          const u32 lo = vhi;
          vhi = missing ? 16 : vhi;
          issue_v_range(s, vrun, lo, vhi);
        }
      }
      if (missing) asm volatile(Q_LOADS Q_OPS);
      const int lane2 = lane_id(), g2 = lane2 >> 4, n2 = lane2 & 15;
      u32 la = s.lbase + lane2 * 16;
      v32u ring; v16u dq;
      asm volatile("ds_read_b128 v[224:227], %2\n\tds_read_b128 v[228:231], %2 offset:1024\n\tds_read_b128 v[232:235], %2 offset:2048\n\tds_read_b128 v[236:239], %2 offset:3072\n\t"
                   "ds_read_b128 v[240:243], %2 offset:4096\n\tds_read_b128 v[244:247], %2 offset:5120\n\tds_read_b128 v[248:251], %2 offset:6144\n\tds_read_b128 v[252:255], %2 offset:7168\n\t"
                   "v_mov_b64 v[208:209], 0\n\tv_mov_b64 v[210:211], 0\n\tv_mov_b64 v[212:213], 0\n\tv_mov_b64 v[214:215], 0\n\t"
                   "v_mov_b64 v[216:217], 0\n\tv_mov_b64 v[218:219], 0\n\tv_mov_b64 v[220:221], 0\n\tv_mov_b64 v[222:223], 0"
                   : "=&{v[224:255]}"(ring), "=&{v[208:223]}"(dq) : "v"(la) : "memory");
      u32 ixq = 0x4444u + (lane2 & 1) * 0xAAAAu;
      PIN(ixq);
      u32 mwa = w * 2048 + (g2 >> 1) * 512 + n2 * 32 + (g2 & 1) * 16, mra = lane2 * 8, moo = (lane2 >> 3) * 256 + (lane2 & 7) * 4;
      char* op = ws + (opo ^ side);
      u32 mla = SC_ML + w * 4;
      PIN(mwa); PIN(mra); PIN(moo); OPAQUE(mla); OPAQUE(op);
      if (missing) {
        asm volatile("s_mov_b32 s98, 0x20000\n.Lq%=:\n\t" Q_LOADS "\n\ts_sub_u32 s98, s98, 1\n\ts_cbranch_scc1 .Lr%=\n\ts_waitcnt vmcnt(4)\n\t" Q_MAX("v206", "v207") "\n\t"
                     "v_cmp_eq_u32 vcc, -1, v206\n\ts_cbranch_vccnz .Lq%=\n.Lr%=:"
                     : BQC("+") : "v"(qoff), "s"(qb), "s"(0x000F000F000F000Full) : "memory", "scc", "vcc", "s98", "v206", "v207");
      }
      float res[4];
      asm volatile("s_waitcnt lgkmcnt(0)\n\t" QK_ASM
                   : "=&{v204}"(res[0]), "=&{v205}"(res[1]), "=&{v206}"(res[2]), "=&{v207}"(res[3]), "+{v[224:255]}"(ring), "+{v[208:223]}"(dq)
                   : "{v[0:7]}"(s.K[0]), "{v[8:15]}"(s.K[1]), "{v[16:23]}"(s.K[2]), "{v[24:31]}"(s.K[3]), "{v[32:39]}"(s.K[4]), "{v[40:47]}"(s.K[5]), "{v[48:55]}"(s.K[6]), "{v[56:63]}"(s.K[7]), "{v[64:71]}"(s.K[8]), "{v[72:79]}"(s.K[9]),
                     "{v[80:87]}"(s.K[10]), "{v[88:95]}"(s.K[11]), "{v[96:103]}"(s.K[12]), "{v[104:111]}"(s.K[13]), BQC(""), "v"(ixq), "v"(la),
                     "s"(0xAAAAAAAAAAAAAAAAull)
                   : ACLOB);
      constexpr float C2 = 0.08838834764831845f * 1.4426950408889634f;
      f32x4 d1, d2;
      u32 m0_, m1_, ixp;
      const float mw = wave_max4(res[0], res[1], res[2], res[3], mra, d1, d2, m0_, m1_, ixp) * C2;
      float pr[4];
#pragma unroll
      for (int z = 0; z < 4; z++) pr[z] = __builtin_amdgcn_exp2f(__builtin_fmaf(res[z], C2, -mw));
      const u32 xa = pk_bf16(pr[0], pr[2]), xb_ = pk_bf16(pr[1], pr[3]);
      float ls = (pr[0] + pr[1]) + (pr[2] + pr[3]), lt;
      v12u pa_;
#define PV_IN : "{v221}"(xa), "{v222}"(xb_), "{v216}"(m0_), "{v217}"(m1_), "{v218}"(ixp) : "memory", ACLOB
      asm volatile(PV_ASM_A : "+{v[208:211]}"(d1), "+{v[212:215]}"(d2), "+{v237}"(ls), "=&{v238}"(lt), [pa] "=&{v[224:235]}"(pa_) PV_IN);
      if (COLD(first)) {
        issue_v_range(s, vrun, vhi, 16);
        VMWAIT(16);
      }
      asm volatile(PV_ASM_B : "+{v[208:211]}"(d1), "+{v[212:215]}"(d2), "+{v237}"(ls), "+{v238}"(lt), [pa] "+{v[224:235]}"(pa_) PV_IN);
      lds_st<float>(mla, mw); lds_st<float>(mla + 16, ls);
      asm volatile(PV_ASM_C : "+{v[208:211]}"(d1), "+{v[212:215]}"(d2), "+{v237}"(ls), "+{v238}"(lt), [pa] "+{v[224:235]}"(pa_) PV_IN);
      asm volatile("" ::BQC(""));
      {
        asm volatile("s_nop 5\n\ts_mov_b32 exec_hi, 0\n\tds_write_b128 %0, %1\n\ts_not_b64 exec, exec\n\tds_write_b128 %0, %2\n\ts_mov_b64 exec, -1\n\t"
                     "s_waitcnt lgkmcnt(0)\n\ts_barrier" ::"v"(mwa), "{v[208:211]}"(d1), "{v[212:215]}"(d2) : "memory", "scc");
        if (w_ == 0) {
          asm volatile("v_mov_b32 v248, 0\n\tds_read_b128 v[208:211], v248 offset:%3\n\tds_read_b128 v[212:215], v248 offset:%4\n\t"
                       "ds_read2st64_b64 v[224:227], %0 offset1:1\n\tds_read2st64_b64 v[228:231], %0 offset0:4 offset1:5\n\t"
                       "ds_read2st64_b64 v[232:235], %0 offset0:8 offset1:9\n\tds_read2st64_b64 v[236:239], %0 offset0:12 offset1:13\n\t"
                       "s_waitcnt lgkmcnt(5)\n\tv_max3_f32 v216, v208, v209, v210\n\tv_max_f32 v216, v216, v211\n\t"
                       "v_sub_f32 v208, v208, v216\n\tv_sub_f32 v209, v209, v216\n\tv_sub_f32 v210, v210, v216\n\tv_sub_f32 v211, v211, v216\n\t"
                       "v_exp_f32 v208, v208\n\tv_exp_f32 v209, v209\n\tv_exp_f32 v210, v210\n\tv_exp_f32 v211, v211\n\t"
                       "s_waitcnt lgkmcnt(2)\n\tv_pk_mul_f32 v[212:213], v[208:209], v[212:213]\n\tv_pk_add_f32 v[224:225], v[224:225], v[226:227]\n\t"
                       "v_pk_fma_f32 v[212:213], v[210:211], v[214:215], v[212:213]\n\tv_pk_add_f32 v[228:229], v[228:229], v[230:231]\n\t"
                       "v_add_f32 v212, v212, v213\n\tv_pk_mul_f32 v[224:225], v[208:209], v[224:225] op_sel_hi:[0,1]\n\tv_rcp_f32 v212, v212\n\t"
                       "s_waitcnt lgkmcnt(0)\n\tv_pk_add_f32 v[232:233], v[232:233], v[234:235]\n\tv_pk_add_f32 v[236:237], v[236:237], v[238:239]\n\t"
                       "v_pk_fma_f32 v[224:225], v[208:209], v[228:229], v[224:225] op_sel:[1,0,0]\n\tv_pk_mul_f32 v[232:233], v[210:211], v[232:233] op_sel_hi:[0,1]\n\ts_nop 0\n\t"
                       "v_pk_fma_f32 v[232:233], v[210:211], v[236:237], v[232:233] op_sel:[1,0,0]\n\ts_nop 0\n\tv_pk_add_f32 v[224:225], v[224:225], v[232:233]\n\ts_nop 0\n\t"
                       "v_pk_mul_f32 v[224:225], v[212:213], v[224:225] op_sel_hi:[0,1]\n\ts_nop 0\n\tv_cvt_pk_bf16_f32 v224, v224, v225\n\tv_min_u32 v224, -2, v224\n\t"
                       "global_store_dword %1, v224, %2 sc1"
                       ::"v"(mra), "v"(moo), "s"(op), "i"(SC_ML), "i"(SC_ML + 16)
                       : "v208", "v209", "v210", "v211", "v212", "v213", "v214", "v215", "v216", "v224", "v225", "v226", "v227", "v228", "v229", "v230", "v231",
                         "v232", "v233", "v234", "v235", "v236", "v237", "v238", "v239", "v248", "memory");
        }
        NAP_IF(w_, CD1);
      }
      {
      const int w = w_;
      if (rk != 0) {
        LANES();
        char* pub = rk >= 4 && !first ? outp : ws + (pubo ^ side);
        const char *gb = ws + (gbo ^ side), *rp = ws + (rpo ^ side);
        OPAQUE(pub);
        u32 po = rk >= 4 && !first ? eb * 1280 + ecol * 2 : rk == 1 ? eb * 32 + ecol * 2 : lane * 4, ra = lane * 8;
        PIN(aux);
        float res_lo = bf_lo(aux), res_hi = bf_hi(aux);
        PIN(po); PIN(ra);
        if (rk == 1) {
          f32x4 t0, t1, t2;
          const u32 xo = (g >> 1) * 512 + (n >> 3) * 256 + (g & 1) * 128 + (n & 7) * 16;
          const bool isG = (g & 1) == ((n >> 2) & 1);
          u32 wa = SC_ROLE + (w * 2 + (n >> 3)) * 512 + (g >> 1) * 256 + (n & 7) * 32 + (g & 1) * 16;
          u32 sa = SC_STAT + w * 256 + (n & 7) * 32 + (isG ? 0 : 16) + (g >> 1) * 8 + (n >> 3) * 4, rs_ = SC_STAT + eb * 32;
          const u32 rc = ecol * 4;
          f32x2 csD = (lds_ld<f32x2>(rc + SC_CSD) + lds_ld<f32x2>(rc + SC_CSD + 64)) + (lds_ld<f32x2>(rc + SC_CSD + 128) + lds_ld<f32x2>(rc + SC_CSD + 192));
          PIN(wa); PIN(sa); PIN(rs_); PIN(csD); PIN(res_lo); PIN(res_hi);
          const u64 mk = ~0ull;
#define DMM(B, WA, C0, C1, C2) MF "%0, " WA ", " B ", " C0 "\n\t" MF "%1, " B ", " B ", " C1 "\n\t" MF "%2, %8, " B ", " C2 "\n\t"
          asm volatile(RING5("", "-2048", "-1024", "0", "1024", "2048", DMM(RB0, "%3", "0", "0", "0"), DMM(RB1, "%4", "%0", "%1", "%2"),
                             DMM(RB2, "%5", "%0", "%1", "%2"), DMM(RB3, "%6", "%0", "%1", "%2"), DMM(RB4, "%7", "%0", "%1", "%2"))
                       : "=&{v[216:219]}"(t0), "=&v"(t1), "=&v"(t2)
                       : WC("", 10), WC("", 11), WC("", 12), WC("", 13), WC("", 14), WC("", 15), [go] "v"(xo), [gb] "s"(gb), [full] "s"(mk), [rf] "s"(rf)
                       : CLOB5);
          lds_st<f32x4>(wa, t0);
          lds_st<float>(sa, lane_sel(t2[0], diag4(t1), 0xF0F00F0FF0F00F0Full));
          lds_barrier();
          if (w == 0) {
            f32x4 g4[4]; f32x2 r4[4], pa[8];
#pragma unroll
            for (int i = 0; i < 4; i++) { g4[i] = lds_ld<f32x4>(rs_ + i * 256); r4[i] = lds_ld<f32x2>(rs_ + i * 256 + 16); }
#pragma unroll
            for (int i = 0; i < 8; i++) pa[i] = lds_ld<f32x2>(ra + SC_ROLE + i * 512);
            GAP();
            const f32x4 gq = (g4[0] + g4[1]) + (g4[2] + g4[3]);
            const f32x2 rq = (r4[0] + r4[1]) + (r4[2] + r4[3]);
            const f32x2 acc = ((pa[0] + pa[1]) + (pa[2] + pa[3])) + ((pa[4] + pa[5]) + (pa[6] + pa[7]));
            const float mean = (rq[0] + rq[1]) * (1.f / 640.f), var = fmax_s(((gq[0] + gq[1]) + (gq[2] + gq[3])) * (1.f / 640.f) - mean * mean, 0.f);
            const float rs = __builtin_amdgcn_rsqf(var + 1e-5f * (var + 1e-5f));
            const float y0 = rs * (acc[0] - mean * csD[0]) + res_lo, y1 = rs * (acc[1] - mean * csD[1]) + res_hi;
            constexpr float GC = -2.f * 0.7978845608028654f * 1.4426950408889634f;
            const float e0 = __builtin_amdgcn_exp2f(y0 * (GC + GC * 0.044715f * y0 * y0)), e1 = __builtin_amdgcn_exp2f(y1 * (GC + GC * 0.044715f * y1 * y1));
            const u32 hv = pk_bf16(y0 * __builtin_amdgcn_rcpf(1.f + e0), y1 * __builtin_amdgcn_rcpf(1.f + e1));
            st_sc1(pub, po, min(hv, SENT - 1));
          }
          NAP_IF(w * (1 - l), QUIET + 3);
        } else {
          f32x2 add = {res_lo, res_hi};
          if (rk >= 4 && w == 0) {
            const u32 p1 = poll_dword(rp, lane * 4), p2 = poll_dword(rp + 256, lane * 4);
            add = f32x2{bf_lo(p1) + bf_lo(p2) + res_lo, bf_hi(p1) + bf_hi(p2) + res_hi};
          }
          f32x4 t0, t1;
          const u32 go = lane * 16;
          u32 wa = (g >> 1) == (n & 1) ? SC_ROLE + (w * 2 + (n & 1)) * 512 + (n >> 1) * 32 + (g & 1) * 16 : SC_DUMP + lane * 16;
          PIN(wa); PIN(add);
          if (rk < 4 && w == 0) __builtin_amdgcn_s_sleep(CD0);
          const u64 mk = ~0ull;
#define CMM(B, WA, WB, C0, C1) MF "%0, " WA ", " B ", " C0 "\n\t" MF "%1, " WB ", " B ", " C1 "\n\t"
          asm volatile(FILL4("v248", "v249", "v250", "v251", "0") FILL4("v252", "v253", "v254", "v255", "0")
                       RHEAD("v224", RB0, "s[80:81]", "-4096", EXACT8, ARM8)
                       RLD(RB7, "s[94:95]", "3072") RLD(RB6, "s[92:93]", "2048") RLD(RB0, "s[80:81]", "-4096") RLD(RB1, "s[82:83]", "-3072")
                       RLD(RB2, "s[84:85]", "-2048") RLD(RB3, "s[86:87]", "-1024") RLD(RB4, "s[88:89]", "0") RLD(RB5, "s[90:91]", "1024")
                       "s_mov_b64 exec, -1\n.Lloop%=:\n\t"
                       RSTEP("7", "v252", RB7, "s[94:95]", "3072", CMM(RB7, "%16", "%17", "0", "0")) RSTEP("7", "v248", RB6, "s[92:93]", "2048", CMM(RB6, "%14", "%15", "%0", "%1"))
                       RSTEP("7", "v224", RB0, "s[80:81]", "-4096", CMM(RB0, "%2", "%3", "%0", "%1")) RSTEP("7", "v228", RB1, "s[82:83]", "-3072", CMM(RB1, "%4", "%5", "%0", "%1"))
                       RSTEP("7", "v232", RB2, "s[84:85]", "-2048", CMM(RB2, "%6", "%7", "%0", "%1")) RSTEP("7", "v236", RB3, "s[86:87]", "-1024", CMM(RB3, "%8", "%9", "%0", "%1"))
                       RSTEP("7", "v240", RB4, "s[88:89]", "0", CMM(RB4, "%10", "%11", "%0", "%1")) OR5
                       "s_or_b64 vcc, s[92:93], s[94:95]\n\ts_or_b64 s[96:97], s[96:97], vcc\n\t"
                       RSTEP("7", "v244", RB5, "s[90:91]", "1024", CMM(RB5, "%12", "%13", "%0", "%1"))
                       "s_nop 1\n\ts_or_b64 s[96:97], s[96:97], s[90:91]\n\t" RTAIL
                       : "=&{v[216:219]}"(t0), "=&v"(t1)
                       : WC("", 0), WC("", 1), WC("", 2), WC("", 3), WC("", 4), WC("", 5), WC("", 6), WC("", 7), WC("", 8), WC("", 9), WC("", 10), WC("", 11),
                         WC("", 12), WC("", 13), WC("", 14), WC("", 15), [go] "v"(go), [gb] "s"(gb), [full] "s"(mk), [rf] "s"(rf), [nf] "s"(nf)
                       : CLOB8);
          lds_st<f32x4>(wa, t0);
          lds_st<f32x4>(wa + 256, t1);
          lds_barrier();
          if (w == 0) {
            f32x2 pa[8];
#pragma unroll
            for (int i = 0; i < 8; i++) pa[i] = lds_ld<f32x2>(ra + SC_ROLE + i * 512);
            const f32x2 acc = (((add + pa[0]) + pa[1]) + (pa[2] + pa[3])) + ((pa[4] + pa[5]) + (pa[6] + pa[7]));
            const u32 xv = min(pk_bf16(acc[0], acc[1]), SENT - 1);
            st_sc1(pub, po, xv);
            if (rk < 4 || first) st_sc1(ws + (pubo ^ side ^ SIDE), po, xv);
            if (rk == 2 && first) aux = poll_dword(rp, lane * 4);
            if (rk == 5 && !first && lane == 0) st_sc1(ws, WS_EPOCH, tag);
          }
          NAP_IF(w * (1 - l), QUIET);
        }
      }
      }
      qbo += Q_L; opo += O_L; pubo += XP_L; rpo += XP_L; gbo += (rk == 2 || rk == 3) ? O_L : XP_L;
      asm volatile("" : "=v"(B[0]), "=v"(B[1]), "=v"(B[2]), "=v"(B[3]), "=v"(B[4]), "=v"(wo_), "=v"(koff));
      asm volatile("" : "=s"(wb), "=s"(wstep), "=s"(wn), "=s"(s.kb), "=s"(s.vb), "=s"(s.tnext), "=s"(tst));
    }
  }
  asm volatile(".Lmega_end:");
}

extern "C" void kernel_launch(void* const* d_in, const int* in_sizes, int n_in, void* d_out, int out_size, void* d_ws,
                              size_t ws_size, hipStream_t stream) {
  (void)hipFuncSetAttribute((const void*)mega, hipFuncAttributeMaxDynamicSharedMemorySize, LDS_BYTES);
  mega<<<256, 256, LDS_BYTES, stream>>>((const char*)d_in[0], (const char*)d_in[1], (const char*)d_in[2], (const char*)d_in[3],
                                        (const char*)d_in[4], (const char*)d_in[6], (const char*)d_in[7], (const char*)d_in[8],
                                        (const char*)d_in[9], (char*)d_out, (char*)d_ws);
}
```
